```python
import torch

def _softmax(x, axis=-1):
    return torch.softmax(x, dim=axis)
def _gelu(x):
    return torch.nn.functional.gelu(x, approximate="tanh")
def _silu(x):
    return torch.nn.functional.silu(x)
def _sigmoid(x):
    return torch.sigmoid(x)
def _layernorm(x, axis=-1, eps=1e-5):
    m = x.mean(dim=axis, keepdim=True)
    v = x.var(dim=axis, keepdim=True, unbiased=False)
    return (x - m) / torch.sqrt(v + eps)
def _scatter_add(tgt, idx, src, axis=0):
    return tgt.clone().index_add_(axis, idx, src)
def _segment_sum(src, seg, n):
    out = torch.zeros((n,) + src.shape[1:], dtype=src.dtype)
    return out.index_add_(0, seg, src)
def _pad(x, pw):
    flat = tuple(v for pair in reversed(pw) for v in pair)
    return torch.nn.functional.pad(x, flat)

kernel_name = 'gated_mix__sliding_block_cached__glu_gated_reglu__decode'

def setup_inputs():

    _w = (lambda a: (lambda t: t.to(torch.bfloat16) if t.is_floating_point() else t)(torch.as_tensor(a)))
    def _A_setup():
        B, Sq, D = 1, 1, 640
        Hq, Hkv, Sk, Dh = 32, 32, 32768, 192
        assert Hq % Hkv == 0, (Hq, Hkv)
        return {
            "x": _w((lambda *s: torch.randn(*s, dtype=torch.bfloat16) * 0.05)(B, Sq, D)),
            "k_cache": _w((lambda *s: torch.randn(*s, dtype=torch.bfloat16) * 6.4)(B, Hkv, Sk, Dh)),
            "v_cache": _w((lambda *s: torch.randn(*s, dtype=torch.bfloat16) * 0.05)(B, Hkv, Sk, Dh)),
            "wq": _w((lambda *s: torch.randn(*s, dtype=torch.bfloat16) * 6.4)(D, Hq * Dh) * (D ** -0.5)),
            "wo": _w((lambda *s: torch.randn(*s, dtype=torch.bfloat16) * 0.05)(Hq * Dh, D) * ((Hq * Dh) ** -0.5)),
        }
    _A_in = _A_setup()

    def _B_setup():
        B, S, D, F = 1, 1, 640, 1536
        return {
            "x": _w((lambda *s: torch.randn(*s, dtype=torch.bfloat16) * 0.05)(B, S, D)),
            "W_gate": _w((lambda *s: torch.randn(*s, dtype=torch.bfloat16) * 0.4)(D, F) * (2.0 / D) ** 0.5),
            "W_up": _w((lambda *s: torch.randn(*s, dtype=torch.bfloat16) * 0.4)(D, F) * (2.0 / D) ** 0.5),
            "W_down": _w((lambda *s: torch.randn(*s, dtype=torch.bfloat16) * 0.4)(F, D) * (1.0 / F) ** 0.5),
        }
    _B_in = _B_setup()

    return {
        "A_x": _A_in["x"],
        "A_k_cache": _A_in["k_cache"],
        "A_v_cache": _A_in["v_cache"],
        "A_wq": _A_in["wq"],
        "A_wo": _A_in["wo"],
        "B_x": _B_in["x"],
        "B_W_gate": _B_in["W_gate"],
        "B_W_up": _B_in["W_up"],
        "B_W_down": _B_in["W_down"],
        "gate_w": _w((lambda *s: torch.randn(*s, dtype=torch.bfloat16) * 0.1)(640) * 16.0),
    }

def reference(A_x, A_k_cache, A_v_cache, A_wq, A_wo, B_x, B_W_gate, B_W_up, B_W_down, gate_w):
    def _A_ref(x, k_cache, v_cache, wq, wo):
        B, Sq, D = x.shape
        Hq, Dh = 32, 192
        Hkv, Sk = k_cache.shape[1], k_cache.shape[2]
        W = min(4096, max(256, Sk // 4))
        rep = Hq // Hkv
        q = (lambda x, *d: x.permute(*d))((x @ wq).reshape(B, Sq, Hq, Dh), 0, 2, 1, 3)
        k = (lambda x, n, axis: torch.repeat_interleave(x, n, dim=axis))(k_cache[:, :, Sk - W:, :], rep, axis=1)
        v = (lambda x, n, axis: torch.repeat_interleave(x, n, dim=axis))(v_cache[:, :, Sk - W:, :], rep, axis=1)
        scores = torch.einsum("bhqd,bhkd->bhqk", q, k) * (Dh ** -0.5)
        p = _softmax(scores, axis=-1)
        o = torch.einsum("bhqk,bhkd->bhqd", p, v)
        return (lambda x, *d: x.permute(*d))(o, 0, 2, 1, 3).reshape(B, Sq, Hq * Dh) @ wo

    def _B_ref(x, W_gate, W_up, W_down):
        gate = torch.einsum("bsd,df->bsf", x, W_gate)
        up = torch.einsum("bsd,df->bsf", x, W_up)
        gate = gate * (lambda x, dt: x.to(dt))(gate > 0, gate.dtype)
        h = gate * up
        return torch.einsum("bsf,fd->bsd", h, W_down)

    x = A_x
    g = _sigmoid((x * gate_w).sum(-1, keepdims=True))
    a = _A_ref(x, A_k_cache, A_v_cache, A_wq, A_wo)
    b = _B_ref(x, B_W_gate, B_W_up, B_W_down)
    return g * a + (1 - g) * b
```

```cpp
#include <hip/hip_runtime.h>
#include <hip/hip_bf16.h>
#include <stdint.h>

constexpr int D = 640, HQ = 32, DH = 192, SK = 32768, WIN = 4096, F = 1536;
constexpr int NSPLIT = 8, KPS = WIN / NSPLIT;

__device__ __forceinline__ float bf2f(unsigned short b) { return __uint_as_float((unsigned)b << 16); }
__device__ __forceinline__ float rbf(float f) { return __bfloat162float(__float2bfloat16(f)); }

__global__ void k1(const unsigned short* x, const unsigned short* wq, const unsigned short* wg, const unsigned short* wu, float* q, float* h) {
  int c = blockIdx.x * blockDim.x + threadIdx.x;
  if (c < HQ * DH) {
    float a = 0.f;
    for (int i = 0; i < D; ++i) a += bf2f(x[i]) * bf2f(wq[(size_t)i * HQ * DH + c]);
    q[c] = rbf(a);
  } else if (c < HQ * DH + F) {
    int f = c - HQ * DH;
    float g = 0.f, u = 0.f;
    for (int i = 0; i < D; ++i) { g += bf2f(x[i]) * bf2f(wg[(size_t)i * F + f]); u += bf2f(x[i]) * bf2f(wu[(size_t)i * F + f]); }
    g = rbf(g); u = rbf(u);
    g = g > 0.f ? g : 0.f;
    h[f] = rbf(g * u);
  }
}
__global__ void k2(const unsigned short* kc, const unsigned short* vc, const float* q, float* po, float* pml) {
  __shared__ float s[KPS];
  __shared__ float red[256];
  __shared__ float qs[DH];
  int hd = blockIdx.x / NSPLIT, sp = blockIdx.x % NSPLIT, t = threadIdx.x;
  if (t < DH) qs[t] = q[hd * DH + t];
  __syncthreads();
  const unsigned short* kb = kc + ((size_t)hd * SK + (SK - WIN) + sp * KPS) * DH;
  const unsigned short* vb = vc + ((size_t)hd * SK + (SK - WIN) + sp * KPS) * DH;
  float mx = -INFINITY;
  for (int k = t; k < KPS; k += 256) {
    float a = 0.f;
    for (int d = 0; d < DH; ++d) a += qs[d] * bf2f(kb[(size_t)k * DH + d]);
    a = rbf(rbf(a) * 0.07216878364870322f);
    s[k] = a; mx = fmaxf(mx, a);
  }
  red[t] = mx; __syncthreads();
  for (int o = 128; o > 0; o >>= 1) { if (t < o) red[t] = fmaxf(red[t], red[t + o]); __syncthreads(); }
  mx = red[0]; __syncthreads();
  float l = 0.f;
  for (int k = t; k < KPS; k += 256) { float p = __expf(s[k] - mx); s[k] = p; l += p; }
  red[t] = l; __syncthreads();
  for (int o = 128; o > 0; o >>= 1) { if (t < o) red[t] += red[t + o]; __syncthreads(); }
  l = red[0];
  if (t < DH) {
    float a = 0.f;
    for (int k = 0; k < KPS; ++k) a += s[k] * bf2f(vb[(size_t)k * DH + t]);
    po[blockIdx.x * DH + t] = a;
  }
  if (t == 0) { pml[2 * blockIdx.x] = mx; pml[2 * blockIdx.x + 1] = l; }
}
__global__ void k3(const float* po, const float* pml, float* o) {
  int hd = blockIdx.x, t = threadIdx.x;
  float M = -INFINITY;
  for (int s = 0; s < NSPLIT; ++s) M = fmaxf(M, pml[2 * (hd * NSPLIT + s)]);
  float L = 0.f, a = 0.f;
  for (int s = 0; s < NSPLIT; ++s) { float w = __expf(pml[2 * (hd * NSPLIT + s)] - M); L += w * pml[2 * (hd * NSPLIT + s) + 1]; a += w * po[(hd * NSPLIT + s) * DH + t]; }
  o[hd * DH + t] = a / L;
}
__global__ void k4(const unsigned short* x, const unsigned short* gw, const float* o, const float* h, const unsigned short* wo, const unsigned short* wd, __hip_bfloat16* out) {
  __shared__ float ra[256], rb[256];
  int j = blockIdx.x, t = threadIdx.x;
  float a = 0.f, b = 0.f;
  for (int i = t; i < HQ * DH; i += 256) a += o[i] * bf2f(wo[(size_t)i * D + j]);
  for (int i = t; i < F; i += 256) b += h[i] * bf2f(wd[(size_t)i * D + j]);
  ra[t] = a; rb[t] = b; __syncthreads();
  for (int of = 128; of > 0; of >>= 1) { if (t < of) { ra[t] += ra[t + of]; rb[t] += rb[t + of]; } __syncthreads(); }
  if (t == 0) {
    float z = 0.f;
    for (int i = 0; i < D; ++i) z += bf2f(x[i]) * bf2f(gw[i]);
    float g = 1.f / (1.f + __expf(-z));
    out[j] = __float2bfloat16(g * ra[0] + (1.f - g) * rb[0]);
  }
}

extern "C" void kernel_launch(void* const* d_in, const int* in_sizes, int n_in,
                              void* d_out, int out_size, void* d_ws, size_t ws_size,
                              hipStream_t stream) {
  const unsigned short* x = (const unsigned short*)d_in[0];
  const unsigned short* kc = (const unsigned short*)d_in[1];
  const unsigned short* vc = (const unsigned short*)d_in[2];
  const unsigned short* wq = (const unsigned short*)d_in[3];
  const unsigned short* wo = (const unsigned short*)d_in[4];
  const unsigned short* wg = (const unsigned short*)d_in[6];
  const unsigned short* wu = (const unsigned short*)d_in[7];
  const unsigned short* wd = (const unsigned short*)d_in[8];
  const unsigned short* gw = (const unsigned short*)d_in[9];
  float* ws = (float*)d_ws;
  float* q = ws; float* h = q + 6144; float* po = h + 1536; float* pml = po + 256 * 192; float* o = pml + 512;
  k1<<<(6144 + 1536) / 64, 64, 0, stream>>>(x, wq, wg, wu, q, h);
  k2<<<256, 256, 0, stream>>>(kc, vc, q, po, pml);
  k3<<<32, 192, 0, stream>>>(po, pml, o);
  k4<<<640, 256, 0, stream>>>(x, gw, o, h, wo, wd, (__hip_bfloat16*)d_out);
}
```

```cpp
#include <hip/hip_runtime.h>
#include <hip/hip_bf16.h>
#include <stdint.h>

#define GAS __attribute__((address_space(1)))
#define LAS __attribute__((address_space(3)))
typedef unsigned v4u __attribute__((ext_vector_type(4)));
typedef unsigned v3u __attribute__((ext_vector_type(3)));
typedef unsigned v2u __attribute__((ext_vector_type(2)));
typedef float v4f __attribute__((ext_vector_type(4)));
typedef __bf16 bf16x2 __attribute__((ext_vector_type(2)));
typedef __amdgpu_buffer_rsrc_t rsrc_t;

#define MAP1 0
#define LSLEEP 1
#define QDELAY 40
#define WO_LAST 1

constexpr int D = 640, HQ = 32, DH = 192, SK = 32768, WIN = 4096, FF = 1536;
constexpr int NWG = 256, NTHR = 1024;
constexpr int NRED = 40;
constexpr int E_OFF = 0;
constexpr int QP_OFF = 4096;
constexpr int GP_OFF = QP_OFF + 32 * 8 * 192 * 8;
constexpr int ML_OFF = GP_OFF + 240 * 1024;
constexpr int OP_OFF = ML_OFF + 32 * 128;
constexpr int YP_OFF = OP_OFF + 32 * 8 * 192 * 8;
constexpr int WS_BYTES = YP_OFF + 256 * 640 * 8;
constexpr int AUX_NT = 2, AUX_SC1 = 16;
constexpr unsigned SPIN_CAP = 1u << 20;

constexpr int L_FLAG = 0;
constexpr int F_Q = 0, F_KMAX = 1, F_P = 2, F_VISS = 3, F_O = 4, F_OPUB = 5, F_SCALE = 6, F_B = 7, F_FIN = 8, F_YPUB = 9, F_KISS = 10;
constexpr int L_Q = 16;
constexpr int L_M = 112;
constexpr int L_L = 120;
constexpr int L_SC = 128;
constexpr int L_BB = 140;
constexpr int L_FIN = 208;
constexpr int L_P = 336;
constexpr int L_O = 848;
constexpr int L_TOTAL = L_O + 8 * 192;

__device__ __forceinline__ rsrc_t mk_rsrc(const void* p, int bytes) {
  return __builtin_amdgcn_make_buffer_rsrc((void*)p, 0, bytes, 0x00020000);
}
__device__ __forceinline__ float bf_lo(unsigned u) { return __uint_as_float(u << 16); }
__device__ __forceinline__ float bf_hi(unsigned u) { return __uint_as_float(u & 0xffff0000u); }
__device__ __forceinline__ float rbf(float f) { return __bfloat162float(__float2bfloat16(f)); }
__device__ __forceinline__ unsigned bfbits(float f) { return (unsigned)__builtin_bit_cast(unsigned short, __float2bfloat16(f)); }
__device__ __forceinline__ float dot2(unsigned a, unsigned b, float c) {
  return __builtin_amdgcn_fdot2_f32_bf16(__builtin_bit_cast(bf16x2, a), __builtin_bit_cast(bf16x2, b), c, false);
}
template <int CTRL> __device__ __forceinline__ float dpp(float x) {
  return __builtin_bit_cast(float, __builtin_amdgcn_mov_dpp(__builtin_bit_cast(int, x), CTRL, 0xf, 0xf, true));
}
constexpr int XOR1 = 0xB1, XOR2 = 0x4E, XOR7 = 0x141, XOR8 = 0x128;
__device__ __forceinline__ float swap16_add(float a, float b) {
  auto s = __builtin_amdgcn_permlane16_swap(__float_as_uint(a), __float_as_uint(b), false, false);
  return __uint_as_float(s[0]) + __uint_as_float(s[1]);
}
__device__ __forceinline__ float swap32_add(float a, float b) {
  auto s = __builtin_amdgcn_permlane32_swap(__float_as_uint(a), __float_as_uint(b), false, false);
  return __uint_as_float(s[0]) + __uint_as_float(s[1]);
}
__device__ __forceinline__ float swap16_max(float a) {
  auto s = __builtin_amdgcn_permlane16_swap(__float_as_uint(a), __float_as_uint(a), false, false);
  return fmaxf(__uint_as_float(s[0]), __uint_as_float(s[1]));
}
__device__ __forceinline__ float swap32_max(float a) {
  auto s = __builtin_amdgcn_permlane32_swap(__float_as_uint(a), __float_as_uint(a), false, false);
  return fmaxf(__uint_as_float(s[0]), __uint_as_float(s[1]));
}
__device__ __forceinline__ float wave_max(float x) {
  x = fmaxf(x, dpp<XOR1>(x)); x = fmaxf(x, dpp<XOR2>(x)); x = fmaxf(x, dpp<XOR7>(x)); x = fmaxf(x, dpp<XOR8>(x));
  x = swap16_max(x); return swap32_max(x);
}
__device__ __forceinline__ float wave_sum(float x) {
  x += dpp<XOR1>(x); x += dpp<XOR2>(x); x += dpp<XOR7>(x); x += dpp<XOR8>(x);
  x = swap16_add(x, x); return swap32_add(x, x);
}
__device__ __forceinline__ void red_rows8(const float (&a)[8], float& r0, float& r1) {
  float z[4];
#pragma unroll
  for (int j = 0; j < 4; ++j) z[j] = swap16_add(a[j], a[j + 4]);
  r0 = swap32_add(z[0], z[2]); r1 = swap32_add(z[1], z[3]);
  r0 += dpp<XOR8>(r0); r1 += dpp<XOR8>(r1);
}
__device__ __forceinline__ void fma8(float (&acc)[8], float s, v4u w) {
#pragma unroll
  for (int j = 0; j < 4; ++j) { acc[2 * j] = fmaf(s, bf_lo(w[j]), acc[2 * j]); acc[2 * j + 1] = fmaf(s, bf_hi(w[j]), acc[2 * j + 1]); }
}
#define WG_BARRIER() do { __builtin_amdgcn_sched_barrier(0); asm volatile("s_barrier" ::: "memory"); __builtin_amdgcn_sched_barrier(0); } while (0)
#define CBAR() asm volatile("" ::: "memory")
#define LGKM0() asm volatile("s_waitcnt lgkmcnt(0)" ::: "memory")
__device__ __forceinline__ unsigned lds_ld(LAS unsigned* p) { return __hip_atomic_load(p, __ATOMIC_RELAXED, __HIP_MEMORY_SCOPE_WORKGROUP); }
__device__ __forceinline__ void lds_st(LAS unsigned* p, unsigned v) { __hip_atomic_store(p, v, __ATOMIC_RELAXED, __HIP_MEMORY_SCOPE_WORKGROUP); }
__device__ __forceinline__ void lds_wait_ge(LAS unsigned* p, unsigned want) {
#pragma unroll 1
  for (unsigned s = 0; s < SPIN_CAP; ++s) {
    CBAR();
    if ((unsigned)__builtin_amdgcn_readfirstlane(lds_ld(p)) >= want) break;
    __builtin_amdgcn_s_sleep(LSLEEP);
  }
  CBAR();
}
__device__ __forceinline__ unsigned lds_arrive(LAS unsigned* p, int lane) {
  LGKM0();
  unsigned old = 0;
  if (lane == 0) old = __hip_atomic_fetch_add(p, 1u, __ATOMIC_RELAXED, __HIP_MEMORY_SCOPE_WORKGROUP);
  old = __builtin_amdgcn_readfirstlane(old);
  CBAR();
  return old;
}
__device__ __forceinline__ void st_gran2(rsrc_t rs, int off, float a, float b, unsigned T) {
  v4u g = {__float_as_uint(a), T, __float_as_uint(b), T};
  __builtin_amdgcn_raw_buffer_store_b128(g, rs, off, 0, AUX_SC1);
}

template <int N, bool PAIR, int SLEEP, bool PRIO = false>
__device__ __forceinline__ void gather(rsrc_t rs, int off, int stride, unsigned T, v4u (&gr)[N]) {
  CBAR();
  if (PRIO) __builtin_amdgcn_s_setprio(3);
#pragma unroll
  for (int i = 0; i < N; ++i) gr[i] = __builtin_amdgcn_raw_buffer_load_b128(rs, off + i * stride, 0, AUX_SC1);
  if (PRIO) { __builtin_amdgcn_sched_barrier(0); __builtin_amdgcn_s_setprio(0); __builtin_amdgcn_sched_barrier(0); }
#pragma unroll 1
  for (unsigned s2 = 0; s2 < SPIN_CAP; ++s2) {
    unsigned bad = 0u;
#pragma unroll
    for (int i = 0; i < N; ++i) bad |= PAIR ? ((gr[i][1] ^ T) | (gr[i][3] ^ T)) : (gr[i][3] ^ T);
    if (__all(bad == 0u)) break;
    __builtin_amdgcn_s_sleep(SLEEP);
    CBAR();
    if (PRIO) __builtin_amdgcn_s_setprio(3);
#pragma unroll
    for (int i = 0; i < N; ++i) {
      const bool have = PAIR ? (((gr[i][1] ^ T) | (gr[i][3] ^ T)) == 0u) : (gr[i][3] == T);
      if (!have) gr[i] = __builtin_amdgcn_raw_buffer_load_b128(rs, off + i * stride, 0, AUX_SC1);
    }
    if (PRIO) { __builtin_amdgcn_sched_barrier(0); __builtin_amdgcn_s_setprio(0); __builtin_amdgcn_sched_barrier(0); }
  }
  CBAR();
}
template <int NI>
__device__ __forceinline__ void gemv_rows(const v4u (&wr)[NI], unsigned xv, int lane, float& r0, float& r1) {
  float acc[8] = {0.f, 0.f, 0.f, 0.f, 0.f, 0.f, 0.f, 0.f};
  const int r = lane >> 3;
#pragma unroll
  for (int i = 0; i < NI; ++i) {
    const unsigned hv = (unsigned)__builtin_amdgcn_ds_bpermute((4 * i + (r >> 1)) * 4, (int)xv);
    fma8(acc, (r & 1) ? bf_hi(hv) : bf_lo(hv), wr[i]);
  }
  red_rows8(acc, r0, r1);
}
#define STAMP(k) do {} while (0)
#define STAMP_FLUSH() do {} while (0)
struct Params {
  const unsigned short *x, *kc, *vc, *wq, *wo, *wg, *wu, *wd, *gw;
  unsigned short* out;
  unsigned char* ws;
};

__global__ void __launch_bounds__(NTHR, 4) fused_decode(Params p) {
  __shared__ __attribute__((aligned(16))) unsigned lds_raw[L_TOTAL];
  LAS unsigned* L = (LAS unsigned*)lds_raw;
  LAS float* Lf = (LAS float*)lds_raw;
  const int b = blockIdx.x, tid = threadIdx.x, lane = tid & 63;
  const int w = __builtin_amdgcn_readfirstlane(tid >> 6);
  STAMP(0);
  if (tid < 16) L[L_FLAG + tid] = 0u;
  LGKM0();
#define WG_BARRIER2() WG_BARRIER()
  const int hd = b >> 3, sp = b & 7;
  const int fc = b / 10, fk = b % 10;
  const bool has_ffn = b < 240;
  const rsrc_t rws = mk_rsrc(p.ws, WS_BYTES);
  const unsigned Eraw = *(const __attribute__((address_space(4))) unsigned*)(p.ws + E_OFF);
#define GET_TAG() ({ unsigned t_; asm volatile("s_add_u32 %0, %1, 1" : "=s"(t_) : "s"(Eraw) : "scc"); t_; })
  const size_t kv_off = ((size_t)hd * SK + (SK - WIN) + sp * 512 + (w & 7) * 64) * DH;
  const int cc2 = 8 * (lane & 7) + 4 * ((lane >> 4) & 1) + 2 * (lane >> 5);

  if (w < 8) {
    if (w < 3) {
      const rsrc_t rq = mk_rsrc(p.wq + (size_t)(80 * sp) * (HQ * DH) + hd * DH + 64 * w, (79 * HQ * DH + 64) * 2);
      const unsigned xq = ((const GAS unsigned*)p.x)[40 * sp + (lane < 40 ? lane : 39)];
      v4u wq_r[10];
#pragma unroll
      for (int i = 0; i < 10; ++i) wq_r[i] = __builtin_amdgcn_raw_buffer_load_b128(rq, (lane >> 3) * (HQ * DH * 2) + (lane & 7) * 16, i * 8 * HQ * DH * 2, AUX_NT);
      WG_BARRIER2();
      float r0, r1;
      gemv_rows<10>(wq_r, xq, lane, r0, r1);
      const unsigned T0 = GET_TAG();
      if (!(lane & 8)) st_gran2(rws, QP_OFF + ((hd * 8 + sp) * 192 + 64 * w + cc2) * 8, r0, r1, T0);
      if (w == 0) {
        STAMP(1);
        const int ln = lane < 48 ? lane : 47;
        float a[4] = {0.f, 0.f, 0.f, 0.f};
        __builtin_amdgcn_s_sleep(QDELAY);
        {
          v4u gr[16];
          gather<16, true, 8>(rws, QP_OFF + hd * 8 * 1536 + ln * 16, 768, T0, gr);
#pragma unroll
          for (int s = 0; s < 8; ++s) {
            a[0] += __uint_as_float(gr[2 * s][0]); a[1] += __uint_as_float(gr[2 * s][2]);
            a[2] += __uint_as_float(gr[2 * s + 1][0]); a[3] += __uint_as_float(gr[2 * s + 1][2]);
          }
        }
        if (lane < 48) {
          L[L_Q + lane] = bfbits(a[0]) | (bfbits(a[1]) << 16);
          L[L_Q + 48 + lane] = bfbits(a[2]) | (bfbits(a[3]) << 16);
        }
        lds_arrive(L + L_FLAG + F_Q, lane);
      }
    } else {
      WG_BARRIER2();
    }
    if (w == 3 && has_ffn) {
      const rsrc_t rg = mk_rsrc(p.wg + (size_t)(64 * fk) * FF + 64 * fc, (63 * FF + 64) * 2);
      const rsrc_t ru = mk_rsrc(p.wu + (size_t)(64 * fk) * FF + 64 * fc, (63 * FF + 64) * 2);
      const unsigned xv = ((const GAS unsigned*)p.x)[32 * fk + (lane & 31)];
      v4u wg[8], wu[8];
#pragma unroll
      for (int i = 0; i < 8; ++i) wg[i] = __builtin_amdgcn_raw_buffer_load_b128(rg, (lane >> 3) * (FF * 2) + (lane & 7) * 16, i * 8 * FF * 2, AUX_NT);
#pragma unroll
      for (int i = 0; i < 8; ++i) wu[i] = __builtin_amdgcn_raw_buffer_load_b128(ru, (lane >> 3) * (FF * 2) + (lane & 7) * 16, i * 8 * FF * 2, AUX_NT);
      __builtin_amdgcn_sched_barrier(0);
      float r0, r1, r2, r3;
      gemv_rows<8>(wg, xv, lane, r0, r1);
      gemv_rows<8>(wu, xv, lane, r2, r3);
      const unsigned T0 = GET_TAG();
      st_gran2(rws, GP_OFF + (fc * 10 + fk) * 1024 + (cc2 + 8 * (lane & 8)) * 8, (lane & 8) ? r2 : r0, (lane & 8) ? r3 : r1, T0);
    }
    if (w != 0) STAMP(1);
    const rsrc_t rk = mk_rsrc(p.kc + kv_off, 64 * DH * 2);
    const int koff = (lane >> 3) * 384 + (lane & 7) * 16;
    v4u kr[24];
#pragma unroll
    for (int g = 0; g < 8; ++g)
#pragma unroll
      for (int t = 0; t < 3; ++t) kr[g * 3 + t] = __builtin_amdgcn_raw_buffer_load_b128(rk, koff + t * 128, g * 3072, AUX_NT);
    __builtin_amdgcn_sched_barrier(0);
    lds_wait_ge(L + L_FLAG + F_Q, 1u);
    STAMP(2);
    v4u qf[3];
#pragma unroll
    for (int t = 0; t < 3; ++t) qf[t] = *(LAS v4u*)(L + L_Q + 32 * t + 4 * (lane & 7));
    float x[8];
#pragma unroll
    for (int g = 0; g < 8; ++g) {
      float a = 0.f;
#pragma unroll
      for (int t = 0; t < 3; ++t)
#pragma unroll
        for (int j = 0; j < 4; ++j) a = dot2(kr[g * 3 + t][j], qf[t][j], a);
      x[g] = a;
    }
#define TR_STEP(HALF, CTRL, BIT) _Pragma("unroll") for (int h = 0; h < (HALF); ++h) { \
      const float lo_ = x[h], hi_ = x[h + (HALF)]; const float t1_ = lo_ + dpp<CTRL>(lo_), t2_ = hi_ + dpp<CTRL>(hi_); \
      x[h] = (lane & (BIT)) ? t2_ : t1_; }
    TR_STEP(4, XOR7, 4) TR_STEP(2, XOR2, 2) TR_STEP(1, XOR1, 1)
#undef TR_STEP
    const float s = rbf(rbf(x[0]) * 0.07216878364870322f);
    const float mw = wave_max(s);
    if (lane == 0) Lf[L_M + w] = mw;
    const unsigned T = GET_TAG();
    STAMP(3);
    lds_arrive(L + L_FLAG + F_KMAX, lane);
    lds_wait_ge(L + L_FLAG + F_KMAX, 8u);
    const v4f m0 = *(LAS v4f*)(Lf + L_M), m1 = *(LAS v4f*)(Lf + L_M + 4);
    const float M = fmaxf(fmaxf(fmaxf(m0[0], m0[1]), fmaxf(m0[2], m0[3])), fmaxf(fmaxf(m1[0], m1[1]), fmaxf(m1[2], m1[3])));
    const float pe = __builtin_amdgcn_exp2f((s - M) * 1.4426950408889634f);
    Lf[L_P + w * 64 + lane] = pe;
    const float lw = wave_sum(pe);
    if (lane == 0) Lf[L_L + w] = lw;
    const unsigned ord = lds_arrive(L + L_FLAG + F_P, lane);
    if (ord == 7u) {
      const v4f l0 = *(LAS v4f*)(Lf + L_L), l1 = *(LAS v4f*)(Lf + L_L + 4);
      const float Ls = ((l0[0] + l0[1]) + (l0[2] + l0[3])) + ((l1[0] + l1[1]) + (l1[2] + l1[3]));
      if (lane == 0) st_gran2(rws, ML_OFF + hd * 128 + sp * 16, M, Ls, T);
    }
    if (w < 5) {
      lds_wait_ge(L + L_FLAG + F_VISS, 8u);
      STAMP(4);
      const int g = lane >> 4, c = lane & 15, half = c >> 3;
      const rsrc_t rwo = mk_rsrc(p.wo + (size_t)(hd * DH + sp * 24) * D, 24 * D * 2);
      v4u wr[6];
#pragma unroll
      for (int i = 0; i < 6; ++i) {
        const int row = 6 * g + 3 * (half ^ (i / 3)) + (i % 3);
        wr[i] = __builtin_amdgcn_raw_buffer_load_b128(rwo, row * (D * 2) + (128 * w + 8 * c) * 2, 0, AUX_NT);
      }
      __builtin_amdgcn_sched_barrier(0);
      const int col = 128 * w + 8 * c + 4 * (g & 1) + 2 * (g >> 1);
      lds_wait_ge(L + L_FLAG + F_SCALE, 1u);
      const float wsp = Lf[L_SC + (c & 7)];
      float y0 = 0.f, y1 = 0.f;
      if (has_ffn) {
        lds_wait_ge(L + L_FLAG + F_B, 1u);
        const int rel = col - 64 * fk;
        const float omg = Lf[L_SC + 8];
        if (rel >= 0 && rel < 64) { y0 = omg * Lf[L_BB + rel]; y1 = omg * Lf[L_BB + rel + 1]; }
      }
      lds_wait_ge(L + L_FLAG + F_OPUB, 1u);
      STAMP(5);
      v4u og[1];
      gather<1, false, 2>(rws, OP_OFF + (((hd * 8 + (c & 7)) * 8 + sp) * 8 + 2 * g + half) * 16, 0, T, og);
      STAMP(6);
      float o6[6];
#pragma unroll
      for (int j = 0; j < 3; ++j) {
        float t = wsp * __uint_as_float(og[0][j]);
        t += dpp<XOR1>(t); t += dpp<XOR2>(t); t += dpp<XOR7>(t);
        o6[j] = t; o6[3 + j] = dpp<XOR8>(t);
      }
      float acc[8] = {0.f, 0.f, 0.f, 0.f, 0.f, 0.f, 0.f, 0.f};
#pragma unroll
      for (int i = 0; i < 6; ++i) fma8(acc, o6[i], wr[i]);
      float z[4];
#pragma unroll
      for (int j = 0; j < 4; ++j) z[j] = swap16_add(acc[j], acc[j + 4]);
      y0 += swap32_add(z[0], z[2]); y1 += swap32_add(z[1], z[3]);
      st_gran2(rws, YP_OFF + (b * 640 + col) * 8, y0, y1, T);
      STAMP(7);
      if (b < NRED) lds_arrive(L + L_FLAG + F_YPUB, lane);
    } else if (w == 5) {
      if (has_ffn) {
        const rsrc_t rwd = mk_rsrc(p.wd + (size_t)(64 * fc) * D + 64 * fk, (63 * D + 64) * 2);
        v4u wr[8];
#pragma unroll
        for (int i = 0; i < 8; ++i) wr[i] = __builtin_amdgcn_raw_buffer_load_b128(rwd, (lane >> 3) * (D * 2) + (lane & 7) * 16, i * 8 * D * 2, AUX_NT);
        float a0 = 0.f, a1 = 0.f;
        {
          v4u gr[10];
          gather<10, true, 8>(rws, GP_OFF + fc * 10 * 1024 + lane * 16, 1024, T, gr);
#pragma unroll
          for (int k = 0; k < 10; ++k) { a0 += __uint_as_float(gr[k][0]); a1 += __uint_as_float(gr[k][2]); }
        }
        a0 = rbf(a0); a1 = rbf(a1);
        auto s0 = __builtin_amdgcn_permlane32_swap(__float_as_uint(a0), __float_as_uint(a0), false, false);
        auto s1 = __builtin_amdgcn_permlane32_swap(__float_as_uint(a1), __float_as_uint(a1), false, false);
        const float g0 = __uint_as_float(s0[0]), u0 = __uint_as_float(s0[1]), g1 = __uint_as_float(s1[0]), u1 = __uint_as_float(s1[1]);
        const unsigned hp = bfbits((g0 > 0.f ? g0 : 0.f) * u0) | (bfbits((g1 > 0.f ? g1 : 0.f) * u1) << 16);
        float r0, r1;
        gemv_rows<8>(wr, hp, lane, r0, r1);
        if (!(lane & 8)) { Lf[L_BB + cc2] = r0; Lf[L_BB + cc2 + 1] = r1; }
        lds_arrive(L + L_FLAG + F_B, lane);
        STAMP(4);
      }
    } else if (w == 6) {
      float z = 0.f;
#pragma unroll
      for (int i = 0; i < 5; ++i) {
        const unsigned xv = ((const GAS unsigned*)p.x)[lane + 64 * i], gv = ((const GAS unsigned*)p.gw)[lane + 64 * i];
        z += rbf(bf_lo(xv) * bf_lo(gv)); z += rbf(bf_hi(xv) * bf_hi(gv));
      }
      z = rbf(wave_sum(z));
      const float gate = rbf(1.f / (1.f + __expf(-z)));
      const float omg = rbf(1.f - gate);
      v4u mg[1];
      gather<1, true, 8>(rws, ML_OFF + hd * 128 + (lane & 7) * 16, 0, T, mg);
      const float ms = __uint_as_float(mg[0][0]), ls = __uint_as_float(mg[0][2]);
      float Mh = ms;
      Mh = fmaxf(Mh, dpp<XOR1>(Mh)); Mh = fmaxf(Mh, dpp<XOR2>(Mh)); Mh = fmaxf(Mh, dpp<XOR7>(Mh));
      const float es = __builtin_amdgcn_exp2f((ms - Mh) * 1.4426950408889634f);
      float Lh = ls * es;
      Lh += dpp<XOR1>(Lh); Lh += dpp<XOR2>(Lh); Lh += dpp<XOR7>(Lh);
      if (lane < 8) Lf[L_SC + lane] = es / Lh * gate;
      if (lane == 0) Lf[L_SC + 8] = omg;
      lds_arrive(L + L_FLAG + F_SCALE, lane);
      STAMP(4);
    }
  } else {
    const int v = w - 8;
    WG_BARRIER2();
    STAMP(1);
    const rsrc_t rv = mk_rsrc(p.vc + kv_off, 64 * DH * 2);
    const int voff = (lane >> 3) * 384 + (lane & 7) * 16;
    v4u vr[24];
#pragma unroll
    for (int t = 0; t < 3; ++t)
#pragma unroll
      for (int g = 0; g < 8; ++g) {
        vr[g * 3 + t] = __builtin_amdgcn_raw_buffer_load_b128(rv, voff + t * 128, g * 3072, AUX_NT);
        __builtin_amdgcn_sched_barrier(0);
      }
    const unsigned T = GET_TAG();
    STAMP(2);
    lds_arrive(L + L_FLAG + F_VISS, lane);
    lds_wait_ge(L + L_FLAG + F_P, 8u);
    STAMP(3);
    {
      const v4f pa = *(LAS v4f*)(Lf + L_P + v * 64 + (lane & ~7)), pb = *(LAS v4f*)(Lf + L_P + v * 64 + (lane & ~7) + 4);
#pragma unroll
      for (int t = 0; t < 3; ++t) {
        float acc[8] = {0.f, 0.f, 0.f, 0.f, 0.f, 0.f, 0.f, 0.f};
#pragma unroll
        for (int g = 0; g < 8; ++g) fma8(acc, g < 4 ? pa[g & 3] : pb[g & 3], vr[g * 3 + t]);
        float r0, r1;
        red_rows8(acc, r0, r1);
        if (!(lane & 8)) { Lf[L_O + v * 192 + 64 * t + cc2] = r0; Lf[L_O + v * 192 + 64 * t + cc2 + 1] = r1; }
        __builtin_amdgcn_sched_barrier(0);
      }
    }
    STAMP(4);
    const unsigned ord = lds_arrive(L + L_FLAG + F_O, lane);
    if (ord == 7u) {
      float s0 = 0.f, s1 = 0.f, s2 = 0.f;
#pragma unroll
      for (int u = 0; u < 8; ++u) { s0 += Lf[L_O + u * 192 + 3 * lane]; s1 += Lf[L_O + u * 192 + 3 * lane + 1]; s2 += Lf[L_O + u * 192 + 3 * lane + 2]; }
      const v4u g3 = {__float_as_uint(s0), __float_as_uint(s1), __float_as_uint(s2), T};
      __builtin_amdgcn_raw_buffer_store_b128(g3, rws, OP_OFF + ((hd * 8 + sp) * 64 + lane) * 16, 0, AUX_SC1);
      lds_arrive(L + L_FLAG + F_OPUB, lane);
      STAMP(5);
    }
    if (b < NRED) {
      lds_wait_ge(L + L_FLAG + F_YPUB, 1u);
      float a0 = 0.f, a1 = 0.f;
      {
        v4u gr[4];
        gather<4, true, 2>(rws, YP_OFF + ((32 * v + (lane >> 3)) * 640 + 16 * b + 2 * (lane & 7)) * 8, 8 * 640 * 8, T, gr);
#pragma unroll
        for (int i = 0; i < 4; ++i) { a0 += __uint_as_float(gr[i][0]); a1 += __uint_as_float(gr[i][2]); }
      }
      STAMP(6);
      a0 += dpp<XOR8>(a0); a1 += dpp<XOR8>(a1);
      a0 = swap16_add(a0, a0); a1 = swap16_add(a1, a1);
      a0 = swap32_add(a0, a0); a1 = swap32_add(a1, a1);
      if (lane < 8) { Lf[L_FIN + v * 16 + 2 * lane] = a0; Lf[L_FIN + v * 16 + 2 * lane + 1] = a1; }
      const unsigned o2 = lds_arrive(L + L_FLAG + F_FIN, lane);
      if (o2 == 7u) {
        if (lane < 8) {
          float s0 = 0.f, s1 = 0.f;
#pragma unroll
          for (int u = 0; u < 8; ++u) { s0 += Lf[L_FIN + u * 16 + 2 * lane]; s1 += Lf[L_FIN + u * 16 + 2 * lane + 1]; }
          ((GAS unsigned*)p.out)[8 * b + lane] = bfbits(s0) | (bfbits(s1) << 16);
        }
        if (b == 0 && lane == 0) *(GAS unsigned*)(p.ws + E_OFF) = T;
        STAMP(7);
      }
    }
  }
  STAMP_FLUSH();
}

extern "C" void kernel_launch(void* const* d_in, const int* in_sizes, int n_in,
                              void* d_out, int out_size, void* d_ws, size_t ws_size,
                              hipStream_t stream) {
  Params p;
  p.x = (const unsigned short*)d_in[0];
  p.kc = (const unsigned short*)d_in[1];
  p.vc = (const unsigned short*)d_in[2];
  p.wq = (const unsigned short*)d_in[3];
  p.wo = (const unsigned short*)d_in[4];
  p.wg = (const unsigned short*)d_in[6];
  p.wu = (const unsigned short*)d_in[7];
  p.wd = (const unsigned short*)d_in[8];
  p.gw = (const unsigned short*)d_in[9];
  p.out = (unsigned short*)d_out;
  p.ws = (unsigned char*)d_ws;
  fused_decode<<<NWG, NTHR, 0, stream>>>(p);
}
```

```cpp
#include <hip/hip_runtime.h>
#include <hip/hip_bf16.h>
#include <stdint.h>
#include <stdio.h>
#include <vector>
#include <algorithm>
#define STAMPS 1

#define GAS __attribute__((address_space(1)))
#define LAS __attribute__((address_space(3)))
typedef unsigned v4u __attribute__((ext_vector_type(4)));
typedef unsigned v3u __attribute__((ext_vector_type(3)));
typedef unsigned v2u __attribute__((ext_vector_type(2)));
typedef float v4f __attribute__((ext_vector_type(4)));
typedef __bf16 bf16x2 __attribute__((ext_vector_type(2)));
typedef __amdgpu_buffer_rsrc_t rsrc_t;

#define MAP1 0
#define LSLEEP 1
#define QDELAY 40
#define WO_LAST 1

constexpr int D = 640, HQ = 32, DH = 192, SK = 32768, WIN = 4096, FF = 1536;
constexpr int NWG = 256, NTHR = 1024;
constexpr int NRED = 40;
constexpr int E_OFF = 0;
constexpr int QP_OFF = 4096;
constexpr int GP_OFF = QP_OFF + 32 * 8 * 192 * 8;
constexpr int ML_OFF = GP_OFF + 240 * 1024;
constexpr int OP_OFF = ML_OFF + 32 * 128;
constexpr int YP_OFF = OP_OFF + 32 * 8 * 192 * 8;
constexpr int WS_BYTES = YP_OFF + 256 * 640 * 8;
constexpr int AUX_NT = 2, AUX_SC1 = 16;
constexpr unsigned SPIN_CAP = 1u << 20;

constexpr int L_FLAG = 0;
constexpr int F_Q = 0, F_KMAX = 1, F_P = 2, F_VISS = 3, F_O = 4, F_OPUB = 5, F_SCALE = 6, F_B = 7, F_FIN = 8, F_YPUB = 9, F_KISS = 10;
constexpr int L_Q = 16;
constexpr int L_M = 112;
constexpr int L_L = 120;
constexpr int L_SC = 128;
constexpr int L_BB = 140;
constexpr int L_FIN = 208;
constexpr int L_P = 336;
constexpr int L_O = 848;
constexpr int L_TOTAL = L_O + 8 * 192;

__device__ __forceinline__ rsrc_t mk_rsrc(const void* p, int bytes) {
  return __builtin_amdgcn_make_buffer_rsrc((void*)p, 0, bytes, 0x00020000);
}
__device__ __forceinline__ float bf_lo(unsigned u) { return __uint_as_float(u << 16); }
__device__ __forceinline__ float bf_hi(unsigned u) { return __uint_as_float(u & 0xffff0000u); }
__device__ __forceinline__ float rbf(float f) { return __bfloat162float(__float2bfloat16(f)); }
__device__ __forceinline__ unsigned bfbits(float f) { return (unsigned)__builtin_bit_cast(unsigned short, __float2bfloat16(f)); }
__device__ __forceinline__ float dot2(unsigned a, unsigned b, float c) {
  return __builtin_amdgcn_fdot2_f32_bf16(__builtin_bit_cast(bf16x2, a), __builtin_bit_cast(bf16x2, b), c, false);
}
template <int CTRL> __device__ __forceinline__ float dpp(float x) {
  return __builtin_bit_cast(float, __builtin_amdgcn_mov_dpp(__builtin_bit_cast(int, x), CTRL, 0xf, 0xf, true));
}
constexpr int XOR1 = 0xB1, XOR2 = 0x4E, XOR7 = 0x141, XOR8 = 0x128;
__device__ __forceinline__ float swap16_add(float a, float b) {
  auto s = __builtin_amdgcn_permlane16_swap(__float_as_uint(a), __float_as_uint(b), false, false);
  return __uint_as_float(s[0]) + __uint_as_float(s[1]);
}
__device__ __forceinline__ float swap32_add(float a, float b) {
  auto s = __builtin_amdgcn_permlane32_swap(__float_as_uint(a), __float_as_uint(b), false, false);
  return __uint_as_float(s[0]) + __uint_as_float(s[1]);
}
__device__ __forceinline__ float swap16_max(float a) {
  auto s = __builtin_amdgcn_permlane16_swap(__float_as_uint(a), __float_as_uint(a), false, false);
  return fmaxf(__uint_as_float(s[0]), __uint_as_float(s[1]));
}
__device__ __forceinline__ float swap32_max(float a) {
  auto s = __builtin_amdgcn_permlane32_swap(__float_as_uint(a), __float_as_uint(a), false, false);
  return fmaxf(__uint_as_float(s[0]), __uint_as_float(s[1]));
}
__device__ __forceinline__ float wave_max(float x) {
  x = fmaxf(x, dpp<XOR1>(x)); x = fmaxf(x, dpp<XOR2>(x)); x = fmaxf(x, dpp<XOR7>(x)); x = fmaxf(x, dpp<XOR8>(x));
  x = swap16_max(x); return swap32_max(x);
}
__device__ __forceinline__ float wave_sum(float x) {
  x += dpp<XOR1>(x); x += dpp<XOR2>(x); x += dpp<XOR7>(x); x += dpp<XOR8>(x);
  x = swap16_add(x, x); return swap32_add(x, x);
}
__device__ __forceinline__ void red_rows8(const float (&a)[8], float& r0, float& r1) {
  float z[4];
#pragma unroll
  for (int j = 0; j < 4; ++j) z[j] = swap16_add(a[j], a[j + 4]);
  r0 = swap32_add(z[0], z[2]); r1 = swap32_add(z[1], z[3]);
  r0 += dpp<XOR8>(r0); r1 += dpp<XOR8>(r1);
}
__device__ __forceinline__ void fma8(float (&acc)[8], float s, v4u w) {
#pragma unroll
  for (int j = 0; j < 4; ++j) { acc[2 * j] = fmaf(s, bf_lo(w[j]), acc[2 * j]); acc[2 * j + 1] = fmaf(s, bf_hi(w[j]), acc[2 * j + 1]); }
}
#define WG_BARRIER() do { __builtin_amdgcn_sched_barrier(0); asm volatile("s_barrier" ::: "memory"); __builtin_amdgcn_sched_barrier(0); } while (0)
#define CBAR() asm volatile("" ::: "memory")
#define LGKM0() asm volatile("s_waitcnt lgkmcnt(0)" ::: "memory")
__device__ __forceinline__ unsigned lds_ld(LAS unsigned* p) { return __hip_atomic_load(p, __ATOMIC_RELAXED, __HIP_MEMORY_SCOPE_WORKGROUP); }
__device__ __forceinline__ void lds_st(LAS unsigned* p, unsigned v) { __hip_atomic_store(p, v, __ATOMIC_RELAXED, __HIP_MEMORY_SCOPE_WORKGROUP); }
__device__ __forceinline__ void lds_wait_ge(LAS unsigned* p, unsigned want) {
#pragma unroll 1
  for (unsigned s = 0; s < SPIN_CAP; ++s) {
    CBAR();
    if ((unsigned)__builtin_amdgcn_readfirstlane(lds_ld(p)) >= want) break;
    __builtin_amdgcn_s_sleep(LSLEEP);
  }
  CBAR();
}
__device__ __forceinline__ unsigned lds_arrive(LAS unsigned* p, int lane) {
  LGKM0();
  unsigned old = 0;
  if (lane == 0) old = __hip_atomic_fetch_add(p, 1u, __ATOMIC_RELAXED, __HIP_MEMORY_SCOPE_WORKGROUP);
  old = __builtin_amdgcn_readfirstlane(old);
  CBAR();
  return old;
}
__device__ __forceinline__ void st_gran2(rsrc_t rs, int off, float a, float b, unsigned T) {
  v4u g = {__float_as_uint(a), T, __float_as_uint(b), T};
  __builtin_amdgcn_raw_buffer_store_b128(g, rs, off, 0, AUX_SC1);
}

template <int N, bool PAIR, int SLEEP, bool PRIO = false>
__device__ __forceinline__ void gather(rsrc_t rs, int off, int stride, unsigned T, v4u (&gr)[N]) {
  CBAR();
  if (PRIO) __builtin_amdgcn_s_setprio(3);
#pragma unroll
  for (int i = 0; i < N; ++i) gr[i] = __builtin_amdgcn_raw_buffer_load_b128(rs, off + i * stride, 0, AUX_SC1);
  if (PRIO) { __builtin_amdgcn_sched_barrier(0); __builtin_amdgcn_s_setprio(0); __builtin_amdgcn_sched_barrier(0); }
#pragma unroll 1
  for (unsigned s2 = 0; s2 < SPIN_CAP; ++s2) {
    unsigned bad = 0u;
#pragma unroll
    for (int i = 0; i < N; ++i) bad |= PAIR ? ((gr[i][1] ^ T) | (gr[i][3] ^ T)) : (gr[i][3] ^ T);
    if (__all(bad == 0u)) break;
    __builtin_amdgcn_s_sleep(SLEEP);
    CBAR();
    if (PRIO) __builtin_amdgcn_s_setprio(3);
#pragma unroll
    for (int i = 0; i < N; ++i) {
      const bool have = PAIR ? (((gr[i][1] ^ T) | (gr[i][3] ^ T)) == 0u) : (gr[i][3] == T);
      if (!have) gr[i] = __builtin_amdgcn_raw_buffer_load_b128(rs, off + i * stride, 0, AUX_SC1);
    }
    if (PRIO) { __builtin_amdgcn_sched_barrier(0); __builtin_amdgcn_s_setprio(0); __builtin_amdgcn_sched_barrier(0); }
  }
  CBAR();
}
template <int NI>
__device__ __forceinline__ void gemv_rows(const v4u (&wr)[NI], unsigned xv, int lane, float& r0, float& r1) {
  float acc[8] = {0.f, 0.f, 0.f, 0.f, 0.f, 0.f, 0.f, 0.f};
  const int r = lane >> 3;
#pragma unroll
  for (int i = 0; i < NI; ++i) {
    const unsigned hv = (unsigned)__builtin_amdgcn_ds_bpermute((4 * i + (r >> 1)) * 4, (int)xv);
    fma8(acc, (r & 1) ? bf_hi(hv) : bf_lo(hv), wr[i]);
  }
  red_rows8(acc, r0, r1);
}
constexpr int NSTAMP = 8;
constexpr size_t STAMP_OFF = 64u << 20;
#define STAMP(k) do { __builtin_amdgcn_sched_barrier(0); stamp_[k] = __builtin_amdgcn_s_memrealtime(); __builtin_amdgcn_sched_barrier(0); } while (0)
#define STAMP_FLUSH() do { if (lane == 0) { unsigned long long* d_ = (unsigned long long*)(p.ws + STAMP_OFF) + (size_t)(b * 16 + w) * NSTAMP; _Pragma("unroll") for (int k_ = 0; k_ < NSTAMP; ++k_) d_[k_] = stamp_[k_]; } } while (0)
struct Params {
  const unsigned short *x, *kc, *vc, *wq, *wo, *wg, *wu, *wd, *gw;
  unsigned short* out;
  unsigned char* ws;
};

__global__ void __launch_bounds__(NTHR, 4) fused_decode(Params p) {
  __shared__ __attribute__((aligned(16))) unsigned lds_raw[L_TOTAL];
  LAS unsigned* L = (LAS unsigned*)lds_raw;
  LAS float* Lf = (LAS float*)lds_raw;
  const int b = blockIdx.x, tid = threadIdx.x, lane = tid & 63;
  const int w = __builtin_amdgcn_readfirstlane(tid >> 6);
  unsigned long long stamp_[NSTAMP] = {0, 0, 0, 0, 0, 0, 0, 0};
  STAMP(0);
  if (tid < 16) L[L_FLAG + tid] = 0u;
  LGKM0();
#define WG_BARRIER2() WG_BARRIER()
  const int hd = b >> 3, sp = b & 7;
  const int fc = b / 10, fk = b % 10;
  const bool has_ffn = b < 240;
  const rsrc_t rws = mk_rsrc(p.ws, WS_BYTES);
  const unsigned Eraw = *(const __attribute__((address_space(4))) unsigned*)(p.ws + E_OFF);
#define GET_TAG() ({ unsigned t_; asm volatile("s_add_u32 %0, %1, 1" : "=s"(t_) : "s"(Eraw) : "scc"); t_; })
  const size_t kv_off = ((size_t)hd * SK + (SK - WIN) + sp * 512 + (w & 7) * 64) * DH;
  const int cc2 = 8 * (lane & 7) + 4 * ((lane >> 4) & 1) + 2 * (lane >> 5);

  if (w < 8) {
    if (w < 3) {
      const rsrc_t rq = mk_rsrc(p.wq + (size_t)(80 * sp) * (HQ * DH) + hd * DH + 64 * w, (79 * HQ * DH + 64) * 2);
      const unsigned xq = ((const GAS unsigned*)p.x)[40 * sp + (lane < 40 ? lane : 39)];
      v4u wq_r[10];
#pragma unroll
      for (int i = 0; i < 10; ++i) wq_r[i] = __builtin_amdgcn_raw_buffer_load_b128(rq, (lane >> 3) * (HQ * DH * 2) + (lane & 7) * 16, i * 8 * HQ * DH * 2, AUX_NT);
      WG_BARRIER2();
      float r0, r1;
      gemv_rows<10>(wq_r, xq, lane, r0, r1);
      const unsigned T0 = GET_TAG();
      if (!(lane & 8)) st_gran2(rws, QP_OFF + ((hd * 8 + sp) * 192 + 64 * w + cc2) * 8, r0, r1, T0);
      if (w == 0) {
        STAMP(1);
        const int ln = lane < 48 ? lane : 47;
        float a[4] = {0.f, 0.f, 0.f, 0.f};
        __builtin_amdgcn_s_sleep(QDELAY);
        {
          v4u gr[16];
          gather<16, true, 8>(rws, QP_OFF + hd * 8 * 1536 + ln * 16, 768, T0, gr);
#pragma unroll
          for (int s = 0; s < 8; ++s) {
            a[0] += __uint_as_float(gr[2 * s][0]); a[1] += __uint_as_float(gr[2 * s][2]);
            a[2] += __uint_as_float(gr[2 * s + 1][0]); a[3] += __uint_as_float(gr[2 * s + 1][2]);
          }
        }
        if (lane < 48) {
          L[L_Q + lane] = bfbits(a[0]) | (bfbits(a[1]) << 16);
          L[L_Q + 48 + lane] = bfbits(a[2]) | (bfbits(a[3]) << 16);
        }
        lds_arrive(L + L_FLAG + F_Q, lane);
      }
    } else {
      WG_BARRIER2();
    }
    if (w == 3 && has_ffn) {
      const rsrc_t rg = mk_rsrc(p.wg + (size_t)(64 * fk) * FF + 64 * fc, (63 * FF + 64) * 2);
      const rsrc_t ru = mk_rsrc(p.wu + (size_t)(64 * fk) * FF + 64 * fc, (63 * FF + 64) * 2);
      const unsigned xv = ((const GAS unsigned*)p.x)[32 * fk + (lane & 31)];
      v4u wg[8], wu[8];
#pragma unroll
      for (int i = 0; i < 8; ++i) wg[i] = __builtin_amdgcn_raw_buffer_load_b128(rg, (lane >> 3) * (FF * 2) + (lane & 7) * 16, i * 8 * FF * 2, AUX_NT);
#pragma unroll
      for (int i = 0; i < 8; ++i) wu[i] = __builtin_amdgcn_raw_buffer_load_b128(ru, (lane >> 3) * (FF * 2) + (lane & 7) * 16, i * 8 * FF * 2, AUX_NT);
      __builtin_amdgcn_sched_barrier(0);
      float r0, r1, r2, r3;
      gemv_rows<8>(wg, xv, lane, r0, r1);
      gemv_rows<8>(wu, xv, lane, r2, r3);
      const unsigned T0 = GET_TAG();
      st_gran2(rws, GP_OFF + (fc * 10 + fk) * 1024 + (cc2 + 8 * (lane & 8)) * 8, (lane & 8) ? r2 : r0, (lane & 8) ? r3 : r1, T0);
    }
    if (w != 0) STAMP(1);
    const rsrc_t rk = mk_rsrc(p.kc + kv_off, 64 * DH * 2);
    const int koff = (lane >> 3) * 384 + (lane & 7) * 16;
    v4u kr[24];
#pragma unroll
    for (int g = 0; g < 8; ++g)
#pragma unroll
      for (int t = 0; t < 3; ++t) kr[g * 3 + t] = __builtin_amdgcn_raw_buffer_load_b128(rk, koff + t * 128, g * 3072, AUX_NT);
    __builtin_amdgcn_sched_barrier(0);
    lds_wait_ge(L + L_FLAG + F_Q, 1u);
    STAMP(2);
    v4u qf[3];
#pragma unroll
    for (int t = 0; t < 3; ++t) qf[t] = *(LAS v4u*)(L + L_Q + 32 * t + 4 * (lane & 7));
    float x[8];
#pragma unroll
    for (int g = 0; g < 8; ++g) {
      float a = 0.f;
#pragma unroll
      for (int t = 0; t < 3; ++t)
#pragma unroll
        for (int j = 0; j < 4; ++j) a = dot2(kr[g * 3 + t][j], qf[t][j], a);
      x[g] = a;
    }
#define TR_STEP(HALF, CTRL, BIT) _Pragma("unroll") for (int h = 0; h < (HALF); ++h) { \
      const float lo_ = x[h], hi_ = x[h + (HALF)]; const float t1_ = lo_ + dpp<CTRL>(lo_), t2_ = hi_ + dpp<CTRL>(hi_); \
      x[h] = (lane & (BIT)) ? t2_ : t1_; }
    TR_STEP(4, XOR7, 4) TR_STEP(2, XOR2, 2) TR_STEP(1, XOR1, 1)
#undef TR_STEP
    const float s = rbf(rbf(x[0]) * 0.07216878364870322f);
    const float mw = wave_max(s);
    if (lane == 0) Lf[L_M + w] = mw;
    const unsigned T = GET_TAG();
    STAMP(3);
    lds_arrive(L + L_FLAG + F_KMAX, lane);
    lds_wait_ge(L + L_FLAG + F_KMAX, 8u);
    const v4f m0 = *(LAS v4f*)(Lf + L_M), m1 = *(LAS v4f*)(Lf + L_M + 4);
    const float M = fmaxf(fmaxf(fmaxf(m0[0], m0[1]), fmaxf(m0[2], m0[3])), fmaxf(fmaxf(m1[0], m1[1]), fmaxf(m1[2], m1[3])));
    const float pe = __builtin_amdgcn_exp2f((s - M) * 1.4426950408889634f);
    Lf[L_P + w * 64 + lane] = pe;
    const float lw = wave_sum(pe);
    if (lane == 0) Lf[L_L + w] = lw;
    const unsigned ord = lds_arrive(L + L_FLAG + F_P, lane);
    if (ord == 7u) {
      const v4f l0 = *(LAS v4f*)(Lf + L_L), l1 = *(LAS v4f*)(Lf + L_L + 4);
      const float Ls = ((l0[0] + l0[1]) + (l0[2] + l0[3])) + ((l1[0] + l1[1]) + (l1[2] + l1[3]));
      if (lane == 0) st_gran2(rws, ML_OFF + hd * 128 + sp * 16, M, Ls, T);
    }
    if (w < 5) {
      lds_wait_ge(L + L_FLAG + F_VISS, 8u);
      STAMP(4);
      const int g = lane >> 4, c = lane & 15, half = c >> 3;
      const rsrc_t rwo = mk_rsrc(p.wo + (size_t)(hd * DH + sp * 24) * D, 24 * D * 2);
      v4u wr[6];
#pragma unroll
      for (int i = 0; i < 6; ++i) {
        const int row = 6 * g + 3 * (half ^ (i / 3)) + (i % 3);
        wr[i] = __builtin_amdgcn_raw_buffer_load_b128(rwo, row * (D * 2) + (128 * w + 8 * c) * 2, 0, AUX_NT);
      }
      __builtin_amdgcn_sched_barrier(0);
      const int col = 128 * w + 8 * c + 4 * (g & 1) + 2 * (g >> 1);
      lds_wait_ge(L + L_FLAG + F_SCALE, 1u);
      const float wsp = Lf[L_SC + (c & 7)];
      float y0 = 0.f, y1 = 0.f;
      if (has_ffn) {
        lds_wait_ge(L + L_FLAG + F_B, 1u);
        const int rel = col - 64 * fk;
        const float omg = Lf[L_SC + 8];
        if (rel >= 0 && rel < 64) { y0 = omg * Lf[L_BB + rel]; y1 = omg * Lf[L_BB + rel + 1]; }
      }
      lds_wait_ge(L + L_FLAG + F_OPUB, 1u);
      STAMP(5);
      v4u og[1];
      gather<1, false, 2>(rws, OP_OFF + (((hd * 8 + (c & 7)) * 8 + sp) * 8 + 2 * g + half) * 16, 0, T, og);
      STAMP(6);
      float o6[6];
#pragma unroll
      for (int j = 0; j < 3; ++j) {
        float t = wsp * __uint_as_float(og[0][j]);
        t += dpp<XOR1>(t); t += dpp<XOR2>(t); t += dpp<XOR7>(t);
        o6[j] = t; o6[3 + j] = dpp<XOR8>(t);
      }
      float acc[8] = {0.f, 0.f, 0.f, 0.f, 0.f, 0.f, 0.f, 0.f};
#pragma unroll
      for (int i = 0; i < 6; ++i) fma8(acc, o6[i], wr[i]);
      float z[4];
#pragma unroll
      for (int j = 0; j < 4; ++j) z[j] = swap16_add(acc[j], acc[j + 4]);
      y0 += swap32_add(z[0], z[2]); y1 += swap32_add(z[1], z[3]);
      st_gran2(rws, YP_OFF + (b * 640 + col) * 8, y0, y1, T);
      STAMP(7);
      if (b < NRED) lds_arrive(L + L_FLAG + F_YPUB, lane);
    } else if (w == 5) {
      if (has_ffn) {
        const rsrc_t rwd = mk_rsrc(p.wd + (size_t)(64 * fc) * D + 64 * fk, (63 * D + 64) * 2);
        v4u wr[8];
#pragma unroll
        for (int i = 0; i < 8; ++i) wr[i] = __builtin_amdgcn_raw_buffer_load_b128(rwd, (lane >> 3) * (D * 2) + (lane & 7) * 16, i * 8 * D * 2, AUX_NT);
        float a0 = 0.f, a1 = 0.f;
        {
          v4u gr[10];
          gather<10, true, 8>(rws, GP_OFF + fc * 10 * 1024 + lane * 16, 1024, T, gr);
#pragma unroll
          for (int k = 0; k < 10; ++k) { a0 += __uint_as_float(gr[k][0]); a1 += __uint_as_float(gr[k][2]); }
        }
        a0 = rbf(a0); a1 = rbf(a1);
        auto s0 = __builtin_amdgcn_permlane32_swap(__float_as_uint(a0), __float_as_uint(a0), false, false);
        auto s1 = __builtin_amdgcn_permlane32_swap(__float_as_uint(a1), __float_as_uint(a1), false, false);
        const float g0 = __uint_as_float(s0[0]), u0 = __uint_as_float(s0[1]), g1 = __uint_as_float(s1[0]), u1 = __uint_as_float(s1[1]);
        const unsigned hp = bfbits((g0 > 0.f ? g0 : 0.f) * u0) | (bfbits((g1 > 0.f ? g1 : 0.f) * u1) << 16);
        float r0, r1;
        gemv_rows<8>(wr, hp, lane, r0, r1);
        if (!(lane & 8)) { Lf[L_BB + cc2] = r0; Lf[L_BB + cc2 + 1] = r1; }
        lds_arrive(L + L_FLAG + F_B, lane);
        STAMP(4);
      }
    } else if (w == 6) {
      float z = 0.f;
#pragma unroll
      for (int i = 0; i < 5; ++i) {
        const unsigned xv = ((const GAS unsigned*)p.x)[lane + 64 * i], gv = ((const GAS unsigned*)p.gw)[lane + 64 * i];
        z += rbf(bf_lo(xv) * bf_lo(gv)); z += rbf(bf_hi(xv) * bf_hi(gv));
      }
      z = rbf(wave_sum(z));
      const float gate = rbf(1.f / (1.f + __expf(-z)));
      const float omg = rbf(1.f - gate);
      v4u mg[1];
      gather<1, true, 8>(rws, ML_OFF + hd * 128 + (lane & 7) * 16, 0, T, mg);
      const float ms = __uint_as_float(mg[0][0]), ls = __uint_as_float(mg[0][2]);
      float Mh = ms;
      Mh = fmaxf(Mh, dpp<XOR1>(Mh)); Mh = fmaxf(Mh, dpp<XOR2>(Mh)); Mh = fmaxf(Mh, dpp<XOR7>(Mh));
      const float es = __builtin_amdgcn_exp2f((ms - Mh) * 1.4426950408889634f);
      float Lh = ls * es;
      Lh += dpp<XOR1>(Lh); Lh += dpp<XOR2>(Lh); Lh += dpp<XOR7>(Lh);
      if (lane < 8) Lf[L_SC + lane] = es / Lh * gate;
      if (lane == 0) Lf[L_SC + 8] = omg;
      lds_arrive(L + L_FLAG + F_SCALE, lane);
      STAMP(4);
    }
  } else {
    const int v = w - 8;
    WG_BARRIER2();
    STAMP(1);
    const rsrc_t rv = mk_rsrc(p.vc + kv_off, 64 * DH * 2);
    const int voff = (lane >> 3) * 384 + (lane & 7) * 16;
    v4u vr[24];
#pragma unroll
    for (int t = 0; t < 3; ++t)
#pragma unroll
      for (int g = 0; g < 8; ++g) {
        vr[g * 3 + t] = __builtin_amdgcn_raw_buffer_load_b128(rv, voff + t * 128, g * 3072, AUX_NT);
        __builtin_amdgcn_sched_barrier(0);
      }
    const unsigned T = GET_TAG();
    STAMP(2);
    lds_arrive(L + L_FLAG + F_VISS, lane);
    lds_wait_ge(L + L_FLAG + F_P, 8u);
    STAMP(3);
    {
      const v4f pa = *(LAS v4f*)(Lf + L_P + v * 64 + (lane & ~7)), pb = *(LAS v4f*)(Lf + L_P + v * 64 + (lane & ~7) + 4);
#pragma unroll
      for (int t = 0; t < 3; ++t) {
        float acc[8] = {0.f, 0.f, 0.f, 0.f, 0.f, 0.f, 0.f, 0.f};
#pragma unroll
        for (int g = 0; g < 8; ++g) fma8(acc, g < 4 ? pa[g & 3] : pb[g & 3], vr[g * 3 + t]);
        float r0, r1;
        red_rows8(acc, r0, r1);
        if (!(lane & 8)) { Lf[L_O + v * 192 + 64 * t + cc2] = r0; Lf[L_O + v * 192 + 64 * t + cc2 + 1] = r1; }
        __builtin_amdgcn_sched_barrier(0);
      }
    }
    STAMP(4);
    const unsigned ord = lds_arrive(L + L_FLAG + F_O, lane);
    if (ord == 7u) {
      float s0 = 0.f, s1 = 0.f, s2 = 0.f;
#pragma unroll
      for (int u = 0; u < 8; ++u) { s0 += Lf[L_O + u * 192 + 3 * lane]; s1 += Lf[L_O + u * 192 + 3 * lane + 1]; s2 += Lf[L_O + u * 192 + 3 * lane + 2]; }
      const v4u g3 = {__float_as_uint(s0), __float_as_uint(s1), __float_as_uint(s2), T};
      __builtin_amdgcn_raw_buffer_store_b128(g3, rws, OP_OFF + ((hd * 8 + sp) * 64 + lane) * 16, 0, AUX_SC1);
      lds_arrive(L + L_FLAG + F_OPUB, lane);
      STAMP(5);
    }
    if (b < NRED) {
      lds_wait_ge(L + L_FLAG + F_YPUB, 1u);
      float a0 = 0.f, a1 = 0.f;
      {
        v4u gr[4];
        gather<4, true, 2>(rws, YP_OFF + ((32 * v + (lane >> 3)) * 640 + 16 * b + 2 * (lane & 7)) * 8, 8 * 640 * 8, T, gr);
#pragma unroll
        for (int i = 0; i < 4; ++i) { a0 += __uint_as_float(gr[i][0]); a1 += __uint_as_float(gr[i][2]); }
      }
      STAMP(6);
      a0 += dpp<XOR8>(a0); a1 += dpp<XOR8>(a1);
      a0 = swap16_add(a0, a0); a1 = swap16_add(a1, a1);
      a0 = swap32_add(a0, a0); a1 = swap32_add(a1, a1);
      if (lane < 8) { Lf[L_FIN + v * 16 + 2 * lane] = a0; Lf[L_FIN + v * 16 + 2 * lane + 1] = a1; }
      const unsigned o2 = lds_arrive(L + L_FLAG + F_FIN, lane);
      if (o2 == 7u) {
        if (lane < 8) {
          float s0 = 0.f, s1 = 0.f;
#pragma unroll
          for (int u = 0; u < 8; ++u) { s0 += Lf[L_FIN + u * 16 + 2 * lane]; s1 += Lf[L_FIN + u * 16 + 2 * lane + 1]; }
          ((GAS unsigned*)p.out)[8 * b + lane] = bfbits(s0) | (bfbits(s1) << 16);
        }
        if (b == 0 && lane == 0) *(GAS unsigned*)(p.ws + E_OFF) = T;
        STAMP(7);
      }
    }
  }
  STAMP_FLUSH();
}

extern "C" void kernel_launch(void* const* d_in, const int* in_sizes, int n_in,
                              void* d_out, int out_size, void* d_ws, size_t ws_size,
                              hipStream_t stream) {
  Params p;
  p.x = (const unsigned short*)d_in[0];
  p.kc = (const unsigned short*)d_in[1];
  p.vc = (const unsigned short*)d_in[2];
  p.wq = (const unsigned short*)d_in[3];
  p.wo = (const unsigned short*)d_in[4];
  p.wg = (const unsigned short*)d_in[6];
  p.wu = (const unsigned short*)d_in[7];
  p.wd = (const unsigned short*)d_in[8];
  p.gw = (const unsigned short*)d_in[9];
  p.out = (unsigned short*)d_out;
  p.ws = (unsigned char*)d_ws;
  fused_decode<<<NWG, NTHR, 0, stream>>>(p);
  static int calls = 0;
  if (++calls == 3) {
    hipStreamSynchronize(stream);
    for (int i = 0; i < n_in; ++i) fprintf(stderr, "in[%d]=%p size %d\n", i, d_in[i], in_sizes[i]);
    fprintf(stderr, "out=%p ws=%p ws_size=%zu\n", d_out, d_ws, ws_size);
    hipEvent_t e0, e1; hipEventCreate(&e0); hipEventCreate(&e1);
    for (int it = 0; it < 12; ++it) {
      const bool cold = it < 8;
      if (cold) { hipMemsetAsync((char*)d_ws + (128u << 20), it, ws_size - (128u << 20), stream); hipStreamSynchronize(stream); }
      hipEventRecord(e0, stream);
      fused_decode<<<NWG, NTHR, 0, stream>>>(p);
      hipEventRecord(e1, stream);
      hipStreamSynchronize(stream);
      float ms; hipEventElapsedTime(&ms, e0, e1);
      std::vector<unsigned long long> hs(256 * 16 * NSTAMP);
      hipMemcpy(hs.data(), (char*)d_ws + STAMP_OFF, hs.size() * 8, hipMemcpyDeviceToHost);
      unsigned long long mn = ~0ull; for (int i = 0; i < 256 * 16; ++i) mn = std::min(mn, hs[i * NSTAMP]);
      fprintf(stderr, "%s ev %.2f |", cold ? "cold" : "warm", ms * 1e3);
      for (int grp = 0; grp < 2; ++grp) { fprintf(stderr, " %s:", grp ? "V" : "K"); for (int k = 0; k < NSTAMP; ++k) { std::vector<double> vals; for (int i = 0; i < 256 * 16; ++i) { int w = i & 15; if ((grp == 0) != (w < 5)) continue; if (grp == 1 && w < 8) continue; unsigned long long t = hs[i * NSTAMP + k]; if (t >= mn && t < mn + 100000) vals.push_back((t - mn) * 0.01); }
          std::sort(vals.begin(), vals.end()); if (vals.empty()) vals.push_back(-1); fprintf(stderr, " [%d]%.1f/%.1f/%.1f", k, vals[0], vals[vals.size() / 2], vals.back()); } }
      fprintf(stderr, "\n");
    }
  }
}
```

```cpp
#include <hip/hip_runtime.h>
#include <hip/hip_bf16.h>
#include <stdint.h>

#define GAS __attribute__((address_space(1)))
#define LAS __attribute__((address_space(3)))
typedef unsigned v4u __attribute__((ext_vector_type(4)));
typedef unsigned v3u __attribute__((ext_vector_type(3)));
typedef unsigned v2u __attribute__((ext_vector_type(2)));
typedef float v4f __attribute__((ext_vector_type(4)));
typedef __bf16 bf16x2 __attribute__((ext_vector_type(2)));
typedef __amdgpu_buffer_rsrc_t rsrc_t;

#define MAP1 0
#define LSLEEP 1
#define QDELAY 40
#define WO_LAST 1

constexpr int D = 640, HQ = 32, DH = 192, SK = 32768, WIN = 4096, FF = 1536;
constexpr int NWG = 256, NTHR = 1024;
constexpr int NRED = 40;
constexpr int E_OFF = 0;
constexpr int QP_OFF = 4096;
constexpr int GP_OFF = QP_OFF + 32 * 8 * 192 * 8;
constexpr int ML_OFF = GP_OFF + 240 * 1024;
constexpr int OP_OFF = ML_OFF + 32 * 128;
constexpr int YP_OFF = OP_OFF + 32 * 8 * 192 * 8;
constexpr int WS_BYTES = YP_OFF + 256 * 640 * 8;
constexpr int AUX_NT = 18, AUX_SC1 = 16;
constexpr unsigned SPIN_CAP = 1u << 20;

constexpr int L_FLAG = 0;
constexpr int F_Q = 0, F_KMAX = 1, F_P = 2, F_VISS = 3, F_O = 4, F_OPUB = 5, F_SCALE = 6, F_B = 7, F_FIN = 8, F_YPUB = 9, F_KISS = 10;
constexpr int L_Q = 16;
constexpr int L_M = 112;
constexpr int L_L = 120;
constexpr int L_SC = 128;
constexpr int L_BB = 140;
constexpr int L_FIN = 208;
constexpr int L_P = 336;
constexpr int L_O = 848;
constexpr int L_TOTAL = L_O + 8 * 192;

__device__ __forceinline__ rsrc_t mk_rsrc(const void* p, int bytes) {
  return __builtin_amdgcn_make_buffer_rsrc((void*)p, 0, bytes, 0x00020000);
}
__device__ __forceinline__ float bf_lo(unsigned u) { return __uint_as_float(u << 16); }
__device__ __forceinline__ float bf_hi(unsigned u) { return __uint_as_float(u & 0xffff0000u); }
__device__ __forceinline__ float rbf(float f) { return __bfloat162float(__float2bfloat16(f)); }
__device__ __forceinline__ unsigned bfbits(float f) { return (unsigned)__builtin_bit_cast(unsigned short, __float2bfloat16(f)); }
__device__ __forceinline__ float dot2(unsigned a, unsigned b, float c) {
  return __builtin_amdgcn_fdot2_f32_bf16(__builtin_bit_cast(bf16x2, a), __builtin_bit_cast(bf16x2, b), c, false);
}
template <int CTRL> __device__ __forceinline__ float dpp(float x) {
  return __builtin_bit_cast(float, __builtin_amdgcn_mov_dpp(__builtin_bit_cast(int, x), CTRL, 0xf, 0xf, true));
}
constexpr int XOR1 = 0xB1, XOR2 = 0x4E, XOR7 = 0x141, XOR8 = 0x128;
__device__ __forceinline__ float swap16_add(float a, float b) {
  auto s = __builtin_amdgcn_permlane16_swap(__float_as_uint(a), __float_as_uint(b), false, false);
  return __uint_as_float(s[0]) + __uint_as_float(s[1]);
}
__device__ __forceinline__ float swap32_add(float a, float b) {
  auto s = __builtin_amdgcn_permlane32_swap(__float_as_uint(a), __float_as_uint(b), false, false);
  return __uint_as_float(s[0]) + __uint_as_float(s[1]);
}
__device__ __forceinline__ float swap16_max(float a) {
  auto s = __builtin_amdgcn_permlane16_swap(__float_as_uint(a), __float_as_uint(a), false, false);
  return fmaxf(__uint_as_float(s[0]), __uint_as_float(s[1]));
}
__device__ __forceinline__ float swap32_max(float a) {
  auto s = __builtin_amdgcn_permlane32_swap(__float_as_uint(a), __float_as_uint(a), false, false);
  return fmaxf(__uint_as_float(s[0]), __uint_as_float(s[1]));
}
__device__ __forceinline__ float wave_max(float x) {
  x = fmaxf(x, dpp<XOR1>(x)); x = fmaxf(x, dpp<XOR2>(x)); x = fmaxf(x, dpp<XOR7>(x)); x = fmaxf(x, dpp<XOR8>(x));
  x = swap16_max(x); return swap32_max(x);
}
__device__ __forceinline__ float wave_sum(float x) {
  x += dpp<XOR1>(x); x += dpp<XOR2>(x); x += dpp<XOR7>(x); x += dpp<XOR8>(x);
  x = swap16_add(x, x); return swap32_add(x, x);
}
__device__ __forceinline__ void red_rows8(const float (&a)[8], float& r0, float& r1) {
  float z[4];
#pragma unroll
  for (int j = 0; j < 4; ++j) z[j] = swap16_add(a[j], a[j + 4]);
  r0 = swap32_add(z[0], z[2]); r1 = swap32_add(z[1], z[3]);
  r0 += dpp<XOR8>(r0); r1 += dpp<XOR8>(r1);
}
__device__ __forceinline__ void fma8(float (&acc)[8], float s, v4u w) {
#pragma unroll
  for (int j = 0; j < 4; ++j) { acc[2 * j] = fmaf(s, bf_lo(w[j]), acc[2 * j]); acc[2 * j + 1] = fmaf(s, bf_hi(w[j]), acc[2 * j + 1]); }
}
#define WG_BARRIER() do { __builtin_amdgcn_sched_barrier(0); asm volatile("s_barrier" ::: "memory"); __builtin_amdgcn_sched_barrier(0); } while (0)
#define CBAR() asm volatile("" ::: "memory")
#define LGKM0() asm volatile("s_waitcnt lgkmcnt(0)" ::: "memory")
__device__ __forceinline__ unsigned lds_ld(LAS unsigned* p) { return __hip_atomic_load(p, __ATOMIC_RELAXED, __HIP_MEMORY_SCOPE_WORKGROUP); }
__device__ __forceinline__ void lds_st(LAS unsigned* p, unsigned v) { __hip_atomic_store(p, v, __ATOMIC_RELAXED, __HIP_MEMORY_SCOPE_WORKGROUP); }
__device__ __forceinline__ void lds_wait_ge(LAS unsigned* p, unsigned want) {
#pragma unroll 1
  for (unsigned s = 0; s < SPIN_CAP; ++s) {
    CBAR();
    if ((unsigned)__builtin_amdgcn_readfirstlane(lds_ld(p)) >= want) break;
    __builtin_amdgcn_s_sleep(LSLEEP);
  }
  CBAR();
}
__device__ __forceinline__ unsigned lds_arrive(LAS unsigned* p, int lane) {
  LGKM0();
  unsigned old = 0;
  if (lane == 0) old = __hip_atomic_fetch_add(p, 1u, __ATOMIC_RELAXED, __HIP_MEMORY_SCOPE_WORKGROUP);
  old = __builtin_amdgcn_readfirstlane(old);
  CBAR();
  return old;
}
__device__ __forceinline__ void st_gran2(rsrc_t rs, int off, float a, float b, unsigned T) {
  v4u g = {__float_as_uint(a), T, __float_as_uint(b), T};
  __builtin_amdgcn_raw_buffer_store_b128(g, rs, off, 0, AUX_SC1);
}

template <int N, bool PAIR, int SLEEP, bool PRIO = false>
__device__ __forceinline__ void gather(rsrc_t rs, int off, int stride, unsigned T, v4u (&gr)[N]) {
  CBAR();
  if (PRIO) __builtin_amdgcn_s_setprio(3);
#pragma unroll
  for (int i = 0; i < N; ++i) gr[i] = __builtin_amdgcn_raw_buffer_load_b128(rs, off + i * stride, 0, AUX_SC1);
  if (PRIO) { __builtin_amdgcn_sched_barrier(0); __builtin_amdgcn_s_setprio(0); __builtin_amdgcn_sched_barrier(0); }
#pragma unroll 1
  for (unsigned s2 = 0; s2 < SPIN_CAP; ++s2) {
    unsigned bad = 0u;
#pragma unroll
    for (int i = 0; i < N; ++i) bad |= PAIR ? ((gr[i][1] ^ T) | (gr[i][3] ^ T)) : (gr[i][3] ^ T);
    if (__all(bad == 0u)) break;
    __builtin_amdgcn_s_sleep(SLEEP);
    CBAR();
    if (PRIO) __builtin_amdgcn_s_setprio(3);
#pragma unroll
    for (int i = 0; i < N; ++i) {
      const bool have = PAIR ? (((gr[i][1] ^ T) | (gr[i][3] ^ T)) == 0u) : (gr[i][3] == T);
      if (!have) gr[i] = __builtin_amdgcn_raw_buffer_load_b128(rs, off + i * stride, 0, AUX_SC1);
    }
    if (PRIO) { __builtin_amdgcn_sched_barrier(0); __builtin_amdgcn_s_setprio(0); __builtin_amdgcn_sched_barrier(0); }
  }
  CBAR();
}
template <int NI>
__device__ __forceinline__ void gemv_rows(const v4u (&wr)[NI], unsigned xv, int lane, float& r0, float& r1) {
  float acc[8] = {0.f, 0.f, 0.f, 0.f, 0.f, 0.f, 0.f, 0.f};
  const int r = lane >> 3;
#pragma unroll
  for (int i = 0; i < NI; ++i) {
    const unsigned hv = (unsigned)__builtin_amdgcn_ds_bpermute((4 * i + (r >> 1)) * 4, (int)xv);
    fma8(acc, (r & 1) ? bf_hi(hv) : bf_lo(hv), wr[i]);
  }
  red_rows8(acc, r0, r1);
}
#define STAMP(k) do {} while (0)
#define STAMP_FLUSH() do {} while (0)
struct Params {
  const unsigned short *x, *kc, *vc, *wq, *wo, *wg, *wu, *wd, *gw;
  unsigned short* out;
  unsigned char* ws;
};

__global__ void __launch_bounds__(NTHR, 4) fused_decode(Params p) {
  __shared__ __attribute__((aligned(16))) unsigned lds_raw[L_TOTAL];
  LAS unsigned* L = (LAS unsigned*)lds_raw;
  LAS float* Lf = (LAS float*)lds_raw;
  const int b = blockIdx.x, tid = threadIdx.x, lane = tid & 63;
  const int w = __builtin_amdgcn_readfirstlane(tid >> 6);
  STAMP(0);
  if (tid < 16) L[L_FLAG + tid] = 0u;
  LGKM0();
#define WG_BARRIER2() WG_BARRIER()
  const int hd = b >> 3, sp = b & 7;
  const int fc = b / 10, fk = b % 10;
  const bool has_ffn = b < 240;
  const rsrc_t rws = mk_rsrc(p.ws, WS_BYTES);
  const unsigned Eraw = *(const __attribute__((address_space(4))) unsigned*)(p.ws + E_OFF);
#define GET_TAG() ({ unsigned t_; asm volatile("s_add_u32 %0, %1, 1" : "=s"(t_) : "s"(Eraw) : "scc"); t_; })
  const size_t kv_off = ((size_t)hd * SK + (SK - WIN) + sp * 512 + (w & 7) * 64) * DH;
  const int cc2 = 8 * (lane & 7) + 4 * ((lane >> 4) & 1) + 2 * (lane >> 5);

  if (w < 8) {
    if (w < 3) {
      const rsrc_t rq = mk_rsrc(p.wq + (size_t)(80 * sp) * (HQ * DH) + hd * DH + 64 * w, (79 * HQ * DH + 64) * 2);
      const unsigned xq = ((const GAS unsigned*)p.x)[40 * sp + (lane < 40 ? lane : 39)];
      v4u wq_r[10];
#pragma unroll
      for (int i = 0; i < 10; ++i) wq_r[i] = __builtin_amdgcn_raw_buffer_load_b128(rq, (lane >> 3) * (HQ * DH * 2) + (lane & 7) * 16, i * 8 * HQ * DH * 2, AUX_NT);
      WG_BARRIER2();
      float r0, r1;
      gemv_rows<10>(wq_r, xq, lane, r0, r1);
      const unsigned T0 = GET_TAG();
      if (!(lane & 8)) st_gran2(rws, QP_OFF + ((hd * 8 + sp) * 192 + 64 * w + cc2) * 8, r0, r1, T0);
      if (w == 0) {
        STAMP(1);
        const int ln = lane < 48 ? lane : 47;
        float a[4] = {0.f, 0.f, 0.f, 0.f};
        __builtin_amdgcn_s_sleep(QDELAY);
        {
          v4u gr[16];
          gather<16, true, 8>(rws, QP_OFF + hd * 8 * 1536 + ln * 16, 768, T0, gr);
#pragma unroll
          for (int s = 0; s < 8; ++s) {
            a[0] += __uint_as_float(gr[2 * s][0]); a[1] += __uint_as_float(gr[2 * s][2]);
            a[2] += __uint_as_float(gr[2 * s + 1][0]); a[3] += __uint_as_float(gr[2 * s + 1][2]);
          }
        }
        if (lane < 48) {
          L[L_Q + lane] = bfbits(a[0]) | (bfbits(a[1]) << 16);
          L[L_Q + 48 + lane] = bfbits(a[2]) | (bfbits(a[3]) << 16);
        }
        lds_arrive(L + L_FLAG + F_Q, lane);
      }
    } else {
      WG_BARRIER2();
    }
    if (w == 3 && has_ffn) {
      const rsrc_t rg = mk_rsrc(p.wg + (size_t)(64 * fk) * FF + 64 * fc, (63 * FF + 64) * 2);
      const rsrc_t ru = mk_rsrc(p.wu + (size_t)(64 * fk) * FF + 64 * fc, (63 * FF + 64) * 2);
      const unsigned xv = ((const GAS unsigned*)p.x)[32 * fk + (lane & 31)];
      v4u wg[8], wu[8];
#pragma unroll
      for (int i = 0; i < 8; ++i) wg[i] = __builtin_amdgcn_raw_buffer_load_b128(rg, (lane >> 3) * (FF * 2) + (lane & 7) * 16, i * 8 * FF * 2, AUX_NT);
#pragma unroll
      for (int i = 0; i < 8; ++i) wu[i] = __builtin_amdgcn_raw_buffer_load_b128(ru, (lane >> 3) * (FF * 2) + (lane & 7) * 16, i * 8 * FF * 2, AUX_NT);
      __builtin_amdgcn_sched_barrier(0);
      float r0, r1, r2, r3;
      gemv_rows<8>(wg, xv, lane, r0, r1);
      gemv_rows<8>(wu, xv, lane, r2, r3);
      const unsigned T0 = GET_TAG();
      st_gran2(rws, GP_OFF + (fc * 10 + fk) * 1024 + (cc2 + 8 * (lane & 8)) * 8, (lane & 8) ? r2 : r0, (lane & 8) ? r3 : r1, T0);
    }
    if (w != 0) STAMP(1);
    const rsrc_t rk = mk_rsrc(p.kc + kv_off, 64 * DH * 2);
    const int koff = (lane >> 3) * 384 + (lane & 7) * 16;
    v4u kr[24];
#pragma unroll
    for (int g = 0; g < 8; ++g)
#pragma unroll
      for (int t = 0; t < 3; ++t) kr[g * 3 + t] = __builtin_amdgcn_raw_buffer_load_b128(rk, koff + t * 128, g * 3072, AUX_NT);
    __builtin_amdgcn_sched_barrier(0);
    lds_wait_ge(L + L_FLAG + F_Q, 1u);
    STAMP(2);
    v4u qf[3];
#pragma unroll
    for (int t = 0; t < 3; ++t) qf[t] = *(LAS v4u*)(L + L_Q + 32 * t + 4 * (lane & 7));
    float x[8];
#pragma unroll
    for (int g = 0; g < 8; ++g) {
      float a = 0.f;
#pragma unroll
      for (int t = 0; t < 3; ++t)
#pragma unroll
        for (int j = 0; j < 4; ++j) a = dot2(kr[g * 3 + t][j], qf[t][j], a);
      x[g] = a;
    }
#define TR_STEP(HALF, CTRL, BIT) _Pragma("unroll") for (int h = 0; h < (HALF); ++h) { \
      const float lo_ = x[h], hi_ = x[h + (HALF)]; const float t1_ = lo_ + dpp<CTRL>(lo_), t2_ = hi_ + dpp<CTRL>(hi_); \
      x[h] = (lane & (BIT)) ? t2_ : t1_; }
    TR_STEP(4, XOR7, 4) TR_STEP(2, XOR2, 2) TR_STEP(1, XOR1, 1)
#undef TR_STEP
    const float s = rbf(rbf(x[0]) * 0.07216878364870322f);
    const float mw = wave_max(s);
    if (lane == 0) Lf[L_M + w] = mw;
    const unsigned T = GET_TAG();
    STAMP(3);
    lds_arrive(L + L_FLAG + F_KMAX, lane);
    lds_wait_ge(L + L_FLAG + F_KMAX, 8u);
    const v4f m0 = *(LAS v4f*)(Lf + L_M), m1 = *(LAS v4f*)(Lf + L_M + 4);
    const float M = fmaxf(fmaxf(fmaxf(m0[0], m0[1]), fmaxf(m0[2], m0[3])), fmaxf(fmaxf(m1[0], m1[1]), fmaxf(m1[2], m1[3])));
    const float pe = __builtin_amdgcn_exp2f((s - M) * 1.4426950408889634f);
    Lf[L_P + w * 64 + lane] = pe;
    const float lw = wave_sum(pe);
    if (lane == 0) Lf[L_L + w] = lw;
    const unsigned ord = lds_arrive(L + L_FLAG + F_P, lane);
    if (ord == 7u) {
      const v4f l0 = *(LAS v4f*)(Lf + L_L), l1 = *(LAS v4f*)(Lf + L_L + 4);
      const float Ls = ((l0[0] + l0[1]) + (l0[2] + l0[3])) + ((l1[0] + l1[1]) + (l1[2] + l1[3]));
      if (lane == 0) st_gran2(rws, ML_OFF + hd * 128 + sp * 16, M, Ls, T);
    }
    if (w < 5) {
      lds_wait_ge(L + L_FLAG + F_VISS, 8u);
      STAMP(4);
      const int g = lane >> 4, c = lane & 15, half = c >> 3;
      const rsrc_t rwo = mk_rsrc(p.wo + (size_t)(hd * DH + sp * 24) * D, 24 * D * 2);
      v4u wr[6];
#pragma unroll
      for (int i = 0; i < 6; ++i) {
        const int row = 6 * g + 3 * (half ^ (i / 3)) + (i % 3);
        wr[i] = __builtin_amdgcn_raw_buffer_load_b128(rwo, row * (D * 2) + (128 * w + 8 * c) * 2, 0, AUX_NT);
      }
      __builtin_amdgcn_sched_barrier(0);
      const int col = 128 * w + 8 * c + 4 * (g & 1) + 2 * (g >> 1);
      lds_wait_ge(L + L_FLAG + F_SCALE, 1u);
      const float wsp = Lf[L_SC + (c & 7)];
      float y0 = 0.f, y1 = 0.f;
      if (has_ffn) {
        lds_wait_ge(L + L_FLAG + F_B, 1u);
        const int rel = col - 64 * fk;
        const float omg = Lf[L_SC + 8];
        if (rel >= 0 && rel < 64) { y0 = omg * Lf[L_BB + rel]; y1 = omg * Lf[L_BB + rel + 1]; }
      }
      lds_wait_ge(L + L_FLAG + F_OPUB, 1u);
      STAMP(5);
      v4u og[1];
      gather<1, false, 2>(rws, OP_OFF + (((hd * 8 + (c & 7)) * 8 + sp) * 8 + 2 * g + half) * 16, 0, T, og);
      STAMP(6);
      float o6[6];
#pragma unroll
      for (int j = 0; j < 3; ++j) {
        float t = wsp * __uint_as_float(og[0][j]);
        t += dpp<XOR1>(t); t += dpp<XOR2>(t); t += dpp<XOR7>(t);
        o6[j] = t; o6[3 + j] = dpp<XOR8>(t);
      }
      float acc[8] = {0.f, 0.f, 0.f, 0.f, 0.f, 0.f, 0.f, 0.f};
#pragma unroll
      for (int i = 0; i < 6; ++i) fma8(acc, o6[i], wr[i]);
      float z[4];
#pragma unroll
      for (int j = 0; j < 4; ++j) z[j] = swap16_add(acc[j], acc[j + 4]);
      y0 += swap32_add(z[0], z[2]); y1 += swap32_add(z[1], z[3]);
      st_gran2(rws, YP_OFF + (b * 640 + col) * 8, y0, y1, T);
      STAMP(7);
      if (b < NRED) lds_arrive(L + L_FLAG + F_YPUB, lane);
    } else if (w == 5) {
      if (has_ffn) {
        const rsrc_t rwd = mk_rsrc(p.wd + (size_t)(64 * fc) * D + 64 * fk, (63 * D + 64) * 2);
        v4u wr[8];
#pragma unroll
        for (int i = 0; i < 8; ++i) wr[i] = __builtin_amdgcn_raw_buffer_load_b128(rwd, (lane >> 3) * (D * 2) + (lane & 7) * 16, i * 8 * D * 2, AUX_NT);
        float a0 = 0.f, a1 = 0.f;
        {
          v4u gr[10];
          gather<10, true, 8>(rws, GP_OFF + fc * 10 * 1024 + lane * 16, 1024, T, gr);
#pragma unroll
          for (int k = 0; k < 10; ++k) { a0 += __uint_as_float(gr[k][0]); a1 += __uint_as_float(gr[k][2]); }
        }
        a0 = rbf(a0); a1 = rbf(a1);
        auto s0 = __builtin_amdgcn_permlane32_swap(__float_as_uint(a0), __float_as_uint(a0), false, false);
        auto s1 = __builtin_amdgcn_permlane32_swap(__float_as_uint(a1), __float_as_uint(a1), false, false);
        const float g0 = __uint_as_float(s0[0]), u0 = __uint_as_float(s0[1]), g1 = __uint_as_float(s1[0]), u1 = __uint_as_float(s1[1]);
        const unsigned hp = bfbits((g0 > 0.f ? g0 : 0.f) * u0) | (bfbits((g1 > 0.f ? g1 : 0.f) * u1) << 16);
        float r0, r1;
        gemv_rows<8>(wr, hp, lane, r0, r1);
        if (!(lane & 8)) { Lf[L_BB + cc2] = r0; Lf[L_BB + cc2 + 1] = r1; }
        lds_arrive(L + L_FLAG + F_B, lane);
        STAMP(4);
      }
    } else if (w == 6) {
      float z = 0.f;
#pragma unroll
      for (int i = 0; i < 5; ++i) {
        const unsigned xv = ((const GAS unsigned*)p.x)[lane + 64 * i], gv = ((const GAS unsigned*)p.gw)[lane + 64 * i];
        z += rbf(bf_lo(xv) * bf_lo(gv)); z += rbf(bf_hi(xv) * bf_hi(gv));
      }
      z = rbf(wave_sum(z));
      const float gate = rbf(1.f / (1.f + __expf(-z)));
      const float omg = rbf(1.f - gate);
      v4u mg[1];
      gather<1, true, 8>(rws, ML_OFF + hd * 128 + (lane & 7) * 16, 0, T, mg);
      const float ms = __uint_as_float(mg[0][0]), ls = __uint_as_float(mg[0][2]);
      float Mh = ms;
      Mh = fmaxf(Mh, dpp<XOR1>(Mh)); Mh = fmaxf(Mh, dpp<XOR2>(Mh)); Mh = fmaxf(Mh, dpp<XOR7>(Mh));
      const float es = __builtin_amdgcn_exp2f((ms - Mh) * 1.4426950408889634f);
      float Lh = ls * es;
      Lh += dpp<XOR1>(Lh); Lh += dpp<XOR2>(Lh); Lh += dpp<XOR7>(Lh);
      if (lane < 8) Lf[L_SC + lane] = es / Lh * gate;
      if (lane == 0) Lf[L_SC + 8] = omg;
      lds_arrive(L + L_FLAG + F_SCALE, lane);
      STAMP(4);
    }
  } else {
    const int v = w - 8;
    WG_BARRIER2();
    STAMP(1);
    const rsrc_t rv = mk_rsrc(p.vc + kv_off, 64 * DH * 2);
    const int voff = (lane >> 3) * 384 + (lane & 7) * 16;
    v4u vr[24];
#pragma unroll
    for (int t = 0; t < 3; ++t)
#pragma unroll
      for (int g = 0; g < 8; ++g) {
        vr[g * 3 + t] = __builtin_amdgcn_raw_buffer_load_b128(rv, voff + t * 128, g * 3072, AUX_NT);
        __builtin_amdgcn_sched_barrier(0);
      }
    const unsigned T = GET_TAG();
    STAMP(2);
    lds_arrive(L + L_FLAG + F_VISS, lane);
    lds_wait_ge(L + L_FLAG + F_P, 8u);
    STAMP(3);
    {
      const v4f pa = *(LAS v4f*)(Lf + L_P + v * 64 + (lane & ~7)), pb = *(LAS v4f*)(Lf + L_P + v * 64 + (lane & ~7) + 4);
#pragma unroll
      for (int t = 0; t < 3; ++t) {
        float acc[8] = {0.f, 0.f, 0.f, 0.f, 0.f, 0.f, 0.f, 0.f};
#pragma unroll
        for (int g = 0; g < 8; ++g) fma8(acc, g < 4 ? pa[g & 3] : pb[g & 3], vr[g * 3 + t]);
        float r0, r1;
        red_rows8(acc, r0, r1);
        if (!(lane & 8)) { Lf[L_O + v * 192 + 64 * t + cc2] = r0; Lf[L_O + v * 192 + 64 * t + cc2 + 1] = r1; }
        __builtin_amdgcn_sched_barrier(0);
      }
    }
    STAMP(4);
    const unsigned ord = lds_arrive(L + L_FLAG + F_O, lane);
    if (ord == 7u) {
      float s0 = 0.f, s1 = 0.f, s2 = 0.f;
#pragma unroll
      for (int u = 0; u < 8; ++u) { s0 += Lf[L_O + u * 192 + 3 * lane]; s1 += Lf[L_O + u * 192 + 3 * lane + 1]; s2 += Lf[L_O + u * 192 + 3 * lane + 2]; }
      const v4u g3 = {__float_as_uint(s0), __float_as_uint(s1), __float_as_uint(s2), T};
      __builtin_amdgcn_raw_buffer_store_b128(g3, rws, OP_OFF + ((hd * 8 + sp) * 64 + lane) * 16, 0, AUX_SC1);
      lds_arrive(L + L_FLAG + F_OPUB, lane);
      STAMP(5);
    }
    if (b < NRED) {
      lds_wait_ge(L + L_FLAG + F_YPUB, 1u);
      float a0 = 0.f, a1 = 0.f;
      {
        v4u gr[4];
        gather<4, true, 2>(rws, YP_OFF + ((32 * v + (lane >> 3)) * 640 + 16 * b + 2 * (lane & 7)) * 8, 8 * 640 * 8, T, gr);
#pragma unroll
        for (int i = 0; i < 4; ++i) { a0 += __uint_as_float(gr[i][0]); a1 += __uint_as_float(gr[i][2]); }
      }
      STAMP(6);
      a0 += dpp<XOR8>(a0); a1 += dpp<XOR8>(a1);
      a0 = swap16_add(a0, a0); a1 = swap16_add(a1, a1);
      a0 = swap32_add(a0, a0); a1 = swap32_add(a1, a1);
      if (lane < 8) { Lf[L_FIN + v * 16 + 2 * lane] = a0; Lf[L_FIN + v * 16 + 2 * lane + 1] = a1; }
      const unsigned o2 = lds_arrive(L + L_FLAG + F_FIN, lane);
      if (o2 == 7u) {
        if (lane < 8) {
          float s0 = 0.f, s1 = 0.f;
#pragma unroll
          for (int u = 0; u < 8; ++u) { s0 += Lf[L_FIN + u * 16 + 2 * lane]; s1 += Lf[L_FIN + u * 16 + 2 * lane + 1]; }
          ((GAS unsigned*)p.out)[8 * b + lane] = bfbits(s0) | (bfbits(s1) << 16);
        }
        if (b == 0 && lane == 0) *(GAS unsigned*)(p.ws + E_OFF) = T;
        STAMP(7);
      }
    }
  }
  STAMP_FLUSH();
}

extern "C" void kernel_launch(void* const* d_in, const int* in_sizes, int n_in,
                              void* d_out, int out_size, void* d_ws, size_t ws_size,
                              hipStream_t stream) {
  Params p;
  p.x = (const unsigned short*)d_in[0];
  p.kc = (const unsigned short*)d_in[1];
  p.vc = (const unsigned short*)d_in[2];
  p.wq = (const unsigned short*)d_in[3];
  p.wo = (const unsigned short*)d_in[4];
  p.wg = (const unsigned short*)d_in[6];
  p.wu = (const unsigned short*)d_in[7];
  p.wd = (const unsigned short*)d_in[8];
  p.gw = (const unsigned short*)d_in[9];
  p.out = (unsigned short*)d_out;
  p.ws = (unsigned char*)d_ws;
  fused_decode<<<NWG, NTHR, 0, stream>>>(p);
}
```

```cpp
#include <hip/hip_runtime.h>
#include <hip/hip_bf16.h>
#include <stdint.h>
#define STAMPS 1
#define LEAK_SEL (w >= 0)
#define LEAK_K 0

#define GAS __attribute__((address_space(1)))
#define LAS __attribute__((address_space(3)))
typedef unsigned v4u __attribute__((ext_vector_type(4)));
typedef unsigned v3u __attribute__((ext_vector_type(3)));
typedef unsigned v2u __attribute__((ext_vector_type(2)));
typedef float v4f __attribute__((ext_vector_type(4)));
typedef __bf16 bf16x2 __attribute__((ext_vector_type(2)));
typedef __amdgpu_buffer_rsrc_t rsrc_t;

#define MAP1 0
#define LSLEEP 1
#define QDELAY 40
#define WO_LAST 1

constexpr int D = 640, HQ = 32, DH = 192, SK = 32768, WIN = 4096, FF = 1536;
constexpr int NWG = 256, NTHR = 1024;
constexpr int NRED = 40;
constexpr int E_OFF = 0;
constexpr int QP_OFF = 4096;
constexpr int GP_OFF = QP_OFF + 32 * 8 * 192 * 8;
constexpr int ML_OFF = GP_OFF + 240 * 1024;
constexpr int OP_OFF = ML_OFF + 32 * 128;
constexpr int YP_OFF = OP_OFF + 32 * 8 * 192 * 8;
constexpr int WS_BYTES = YP_OFF + 256 * 640 * 8;
constexpr int AUX_NT = 2, AUX_SC1 = 16;
constexpr unsigned SPIN_CAP = 1u << 20;

constexpr int L_FLAG = 0;
constexpr int F_Q = 0, F_KMAX = 1, F_P = 2, F_VISS = 3, F_O = 4, F_OPUB = 5, F_SCALE = 6, F_B = 7, F_FIN = 8, F_YPUB = 9, F_KISS = 10;
constexpr int L_Q = 16;
constexpr int L_M = 112;
constexpr int L_L = 120;
constexpr int L_SC = 128;
constexpr int L_BB = 140;
constexpr int L_FIN = 208;
constexpr int L_P = 336;
constexpr int L_O = 848;
constexpr int L_TOTAL = L_O + 8 * 192;

__device__ __forceinline__ rsrc_t mk_rsrc(const void* p, int bytes) {
  return __builtin_amdgcn_make_buffer_rsrc((void*)p, 0, bytes, 0x00020000);
}
__device__ __forceinline__ float bf_lo(unsigned u) { return __uint_as_float(u << 16); }
__device__ __forceinline__ float bf_hi(unsigned u) { return __uint_as_float(u & 0xffff0000u); }
__device__ __forceinline__ float rbf(float f) { return __bfloat162float(__float2bfloat16(f)); }
__device__ __forceinline__ unsigned bfbits(float f) { return (unsigned)__builtin_bit_cast(unsigned short, __float2bfloat16(f)); }
__device__ __forceinline__ float dot2(unsigned a, unsigned b, float c) {
  return __builtin_amdgcn_fdot2_f32_bf16(__builtin_bit_cast(bf16x2, a), __builtin_bit_cast(bf16x2, b), c, false);
}
template <int CTRL> __device__ __forceinline__ float dpp(float x) {
  return __builtin_bit_cast(float, __builtin_amdgcn_mov_dpp(__builtin_bit_cast(int, x), CTRL, 0xf, 0xf, true));
}
constexpr int XOR1 = 0xB1, XOR2 = 0x4E, XOR7 = 0x141, XOR8 = 0x128;
__device__ __forceinline__ float swap16_add(float a, float b) {
  auto s = __builtin_amdgcn_permlane16_swap(__float_as_uint(a), __float_as_uint(b), false, false);
  return __uint_as_float(s[0]) + __uint_as_float(s[1]);
}
__device__ __forceinline__ float swap32_add(float a, float b) {
  auto s = __builtin_amdgcn_permlane32_swap(__float_as_uint(a), __float_as_uint(b), false, false);
  return __uint_as_float(s[0]) + __uint_as_float(s[1]);
}
__device__ __forceinline__ float swap16_max(float a) {
  auto s = __builtin_amdgcn_permlane16_swap(__float_as_uint(a), __float_as_uint(a), false, false);
  return fmaxf(__uint_as_float(s[0]), __uint_as_float(s[1]));
}
__device__ __forceinline__ float swap32_max(float a) {
  auto s = __builtin_amdgcn_permlane32_swap(__float_as_uint(a), __float_as_uint(a), false, false);
  return fmaxf(__uint_as_float(s[0]), __uint_as_float(s[1]));
}
__device__ __forceinline__ float wave_max(float x) {
  x = fmaxf(x, dpp<XOR1>(x)); x = fmaxf(x, dpp<XOR2>(x)); x = fmaxf(x, dpp<XOR7>(x)); x = fmaxf(x, dpp<XOR8>(x));
  x = swap16_max(x); return swap32_max(x);
}
__device__ __forceinline__ float wave_sum(float x) {
  x += dpp<XOR1>(x); x += dpp<XOR2>(x); x += dpp<XOR7>(x); x += dpp<XOR8>(x);
  x = swap16_add(x, x); return swap32_add(x, x);
}
__device__ __forceinline__ void red_rows8(const float (&a)[8], float& r0, float& r1) {
  float z[4];
#pragma unroll
  for (int j = 0; j < 4; ++j) z[j] = swap16_add(a[j], a[j + 4]);
  r0 = swap32_add(z[0], z[2]); r1 = swap32_add(z[1], z[3]);
  r0 += dpp<XOR8>(r0); r1 += dpp<XOR8>(r1);
}
__device__ __forceinline__ void fma8(float (&acc)[8], float s, v4u w) {
#pragma unroll
  for (int j = 0; j < 4; ++j) { acc[2 * j] = fmaf(s, bf_lo(w[j]), acc[2 * j]); acc[2 * j + 1] = fmaf(s, bf_hi(w[j]), acc[2 * j + 1]); }
}
#define WG_BARRIER() do { __builtin_amdgcn_sched_barrier(0); asm volatile("s_barrier" ::: "memory"); __builtin_amdgcn_sched_barrier(0); } while (0)
#define CBAR() asm volatile("" ::: "memory")
#define LGKM0() asm volatile("s_waitcnt lgkmcnt(0)" ::: "memory")
__device__ __forceinline__ unsigned lds_ld(LAS unsigned* p) { return __hip_atomic_load(p, __ATOMIC_RELAXED, __HIP_MEMORY_SCOPE_WORKGROUP); }
__device__ __forceinline__ void lds_st(LAS unsigned* p, unsigned v) { __hip_atomic_store(p, v, __ATOMIC_RELAXED, __HIP_MEMORY_SCOPE_WORKGROUP); }
__device__ __forceinline__ void lds_wait_ge(LAS unsigned* p, unsigned want) {
#pragma unroll 1
  for (unsigned s = 0; s < SPIN_CAP; ++s) {
    CBAR();
    if ((unsigned)__builtin_amdgcn_readfirstlane(lds_ld(p)) >= want) break;
    __builtin_amdgcn_s_sleep(LSLEEP);
  }
  CBAR();
}
__device__ __forceinline__ unsigned lds_arrive(LAS unsigned* p, int lane) {
  LGKM0();
  unsigned old = 0;
  if (lane == 0) old = __hip_atomic_fetch_add(p, 1u, __ATOMIC_RELAXED, __HIP_MEMORY_SCOPE_WORKGROUP);
  old = __builtin_amdgcn_readfirstlane(old);
  CBAR();
  return old;
}
__device__ __forceinline__ void st_gran2(rsrc_t rs, int off, float a, float b, unsigned T) {
  v4u g = {__float_as_uint(a), T, __float_as_uint(b), T};
  __builtin_amdgcn_raw_buffer_store_b128(g, rs, off, 0, AUX_SC1);
}

template <int N, bool PAIR, int SLEEP, bool PRIO = false>
__device__ __forceinline__ void gather(rsrc_t rs, int off, int stride, unsigned T, v4u (&gr)[N]) {
  CBAR();
  if (PRIO) __builtin_amdgcn_s_setprio(3);
#pragma unroll
  for (int i = 0; i < N; ++i) gr[i] = __builtin_amdgcn_raw_buffer_load_b128(rs, off + i * stride, 0, AUX_SC1);
  if (PRIO) { __builtin_amdgcn_sched_barrier(0); __builtin_amdgcn_s_setprio(0); __builtin_amdgcn_sched_barrier(0); }
#pragma unroll 1
  for (unsigned s2 = 0; s2 < SPIN_CAP; ++s2) {
    unsigned bad = 0u;
#pragma unroll
    for (int i = 0; i < N; ++i) bad |= PAIR ? ((gr[i][1] ^ T) | (gr[i][3] ^ T)) : (gr[i][3] ^ T);
    if (__all(bad == 0u)) break;
    __builtin_amdgcn_s_sleep(SLEEP);
    CBAR();
    if (PRIO) __builtin_amdgcn_s_setprio(3);
#pragma unroll
    for (int i = 0; i < N; ++i) {
      const bool have = PAIR ? (((gr[i][1] ^ T) | (gr[i][3] ^ T)) == 0u) : (gr[i][3] == T);
      if (!have) gr[i] = __builtin_amdgcn_raw_buffer_load_b128(rs, off + i * stride, 0, AUX_SC1);
    }
    if (PRIO) { __builtin_amdgcn_sched_barrier(0); __builtin_amdgcn_s_setprio(0); __builtin_amdgcn_sched_barrier(0); }
  }
  CBAR();
}
template <int NI>
__device__ __forceinline__ void gemv_rows(const v4u (&wr)[NI], unsigned xv, int lane, float& r0, float& r1) {
  float acc[8] = {0.f, 0.f, 0.f, 0.f, 0.f, 0.f, 0.f, 0.f};
  const int r = lane >> 3;
#pragma unroll
  for (int i = 0; i < NI; ++i) {
    const unsigned hv = (unsigned)__builtin_amdgcn_ds_bpermute((4 * i + (r >> 1)) * 4, (int)xv);
    fma8(acc, (r & 1) ? bf_hi(hv) : bf_lo(hv), wr[i]);
  }
  red_rows8(acc, r0, r1);
}
constexpr int NSTAMP = 8;
constexpr size_t STAMP_OFF = 64u << 20;
#define STAMP(k) do { __builtin_amdgcn_sched_barrier(0); stamp_[k] = __builtin_amdgcn_s_memrealtime(); __builtin_amdgcn_sched_barrier(0); } while (0)
#define STAMP_FLUSH() do { if (lane == 0) { unsigned long long* d_ = (unsigned long long*)(p.ws + STAMP_OFF) + (size_t)(b * 16 + w) * NSTAMP; _Pragma("unroll") for (int k_ = 0; k_ < NSTAMP; ++k_) d_[k_] = stamp_[k_]; } } while (0)
struct Params {
  const unsigned short *x, *kc, *vc, *wq, *wo, *wg, *wu, *wd, *gw;
  unsigned short* out;
  unsigned char* ws;
};

__global__ void __launch_bounds__(NTHR, 4) fused_decode(Params p) {
  __shared__ __attribute__((aligned(16))) unsigned lds_raw[L_TOTAL];
  LAS unsigned* L = (LAS unsigned*)lds_raw;
  LAS float* Lf = (LAS float*)lds_raw;
  const int b = blockIdx.x, tid = threadIdx.x, lane = tid & 63;
  const int w = __builtin_amdgcn_readfirstlane(tid >> 6);
  unsigned long long stamp_[NSTAMP] = {0, 0, 0, 0, 0, 0, 0, 0};
  STAMP(0);
  if (tid < 16) L[L_FLAG + tid] = 0u;
  LGKM0();
#define WG_BARRIER2() WG_BARRIER()
  const int hd = b >> 3, sp = b & 7;
  const int fc = b / 10, fk = b % 10;
  const bool has_ffn = b < 240;
  const rsrc_t rws = mk_rsrc(p.ws, WS_BYTES);
  const unsigned Eraw = *(const __attribute__((address_space(4))) unsigned*)(p.ws + E_OFF);
#define GET_TAG() ({ unsigned t_; asm volatile("s_add_u32 %0, %1, 1" : "=s"(t_) : "s"(Eraw) : "scc"); t_; })
  const size_t kv_off = ((size_t)hd * SK + (SK - WIN) + sp * 512 + (w & 7) * 64) * DH;
  const int cc2 = 8 * (lane & 7) + 4 * ((lane >> 4) & 1) + 2 * (lane >> 5);

  if (w < 8) {
    if (w < 3) {
      const rsrc_t rq = mk_rsrc(p.wq + (size_t)(80 * sp) * (HQ * DH) + hd * DH + 64 * w, (79 * HQ * DH + 64) * 2);
      const unsigned xq = ((const GAS unsigned*)p.x)[40 * sp + (lane < 40 ? lane : 39)];
      v4u wq_r[10];
#pragma unroll
      for (int i = 0; i < 10; ++i) wq_r[i] = __builtin_amdgcn_raw_buffer_load_b128(rq, (lane >> 3) * (HQ * DH * 2) + (lane & 7) * 16, i * 8 * HQ * DH * 2, AUX_NT);
      WG_BARRIER2();
      float r0, r1;
      gemv_rows<10>(wq_r, xq, lane, r0, r1);
      const unsigned T0 = GET_TAG();
      if (!(lane & 8)) st_gran2(rws, QP_OFF + ((hd * 8 + sp) * 192 + 64 * w + cc2) * 8, r0, r1, T0);
      if (w == 0) {
        STAMP(1);
        const int ln = lane < 48 ? lane : 47;
        float a[4] = {0.f, 0.f, 0.f, 0.f};
        __builtin_amdgcn_s_sleep(QDELAY);
        {
          v4u gr[16];
          gather<16, true, 8>(rws, QP_OFF + hd * 8 * 1536 + ln * 16, 768, T0, gr);
#pragma unroll
          for (int s = 0; s < 8; ++s) {
            a[0] += __uint_as_float(gr[2 * s][0]); a[1] += __uint_as_float(gr[2 * s][2]);
            a[2] += __uint_as_float(gr[2 * s + 1][0]); a[3] += __uint_as_float(gr[2 * s + 1][2]);
          }
        }
        if (lane < 48) {
          L[L_Q + lane] = bfbits(a[0]) | (bfbits(a[1]) << 16);
          L[L_Q + 48 + lane] = bfbits(a[2]) | (bfbits(a[3]) << 16);
        }
        lds_arrive(L + L_FLAG + F_Q, lane);
      }
    } else {
      WG_BARRIER2();
    }
    if (w == 3 && has_ffn) {
      const rsrc_t rg = mk_rsrc(p.wg + (size_t)(64 * fk) * FF + 64 * fc, (63 * FF + 64) * 2);
      const rsrc_t ru = mk_rsrc(p.wu + (size_t)(64 * fk) * FF + 64 * fc, (63 * FF + 64) * 2);
      const unsigned xv = ((const GAS unsigned*)p.x)[32 * fk + (lane & 31)];
      v4u wg[8], wu[8];
#pragma unroll
      for (int i = 0; i < 8; ++i) wg[i] = __builtin_amdgcn_raw_buffer_load_b128(rg, (lane >> 3) * (FF * 2) + (lane & 7) * 16, i * 8 * FF * 2, AUX_NT);
#pragma unroll
      for (int i = 0; i < 8; ++i) wu[i] = __builtin_amdgcn_raw_buffer_load_b128(ru, (lane >> 3) * (FF * 2) + (lane & 7) * 16, i * 8 * FF * 2, AUX_NT);
      __builtin_amdgcn_sched_barrier(0);
      float r0, r1, r2, r3;
      gemv_rows<8>(wg, xv, lane, r0, r1);
      gemv_rows<8>(wu, xv, lane, r2, r3);
      const unsigned T0 = GET_TAG();
      st_gran2(rws, GP_OFF + (fc * 10 + fk) * 1024 + (cc2 + 8 * (lane & 8)) * 8, (lane & 8) ? r2 : r0, (lane & 8) ? r3 : r1, T0);
    }
    if (w != 0) STAMP(1);
    const rsrc_t rk = mk_rsrc(p.kc + kv_off, 64 * DH * 2);
    const int koff = (lane >> 3) * 384 + (lane & 7) * 16;
    v4u kr[24];
#pragma unroll
    for (int g = 0; g < 8; ++g)
#pragma unroll
      for (int t = 0; t < 3; ++t) kr[g * 3 + t] = __builtin_amdgcn_raw_buffer_load_b128(rk, koff + t * 128, g * 3072, AUX_NT);
    __builtin_amdgcn_sched_barrier(0);
    lds_wait_ge(L + L_FLAG + F_Q, 1u);
    STAMP(2);
    v4u qf[3];
#pragma unroll
    for (int t = 0; t < 3; ++t) qf[t] = *(LAS v4u*)(L + L_Q + 32 * t + 4 * (lane & 7));
    float x[8];
#pragma unroll
    for (int g = 0; g < 8; ++g) {
      float a = 0.f;
#pragma unroll
      for (int t = 0; t < 3; ++t)
#pragma unroll
        for (int j = 0; j < 4; ++j) a = dot2(kr[g * 3 + t][j], qf[t][j], a);
      x[g] = a;
    }
#define TR_STEP(HALF, CTRL, BIT) _Pragma("unroll") for (int h = 0; h < (HALF); ++h) { \
      const float lo_ = x[h], hi_ = x[h + (HALF)]; const float t1_ = lo_ + dpp<CTRL>(lo_), t2_ = hi_ + dpp<CTRL>(hi_); \
      x[h] = (lane & (BIT)) ? t2_ : t1_; }
    TR_STEP(4, XOR7, 4) TR_STEP(2, XOR2, 2) TR_STEP(1, XOR1, 1)
#undef TR_STEP
    const float s = rbf(rbf(x[0]) * 0.07216878364870322f);
    const float mw = wave_max(s);
    if (lane == 0) Lf[L_M + w] = mw;
    const unsigned T = GET_TAG();
    STAMP(3);
    lds_arrive(L + L_FLAG + F_KMAX, lane);
    lds_wait_ge(L + L_FLAG + F_KMAX, 8u);
    const v4f m0 = *(LAS v4f*)(Lf + L_M), m1 = *(LAS v4f*)(Lf + L_M + 4);
    const float M = fmaxf(fmaxf(fmaxf(m0[0], m0[1]), fmaxf(m0[2], m0[3])), fmaxf(fmaxf(m1[0], m1[1]), fmaxf(m1[2], m1[3])));
    const float pe = __builtin_amdgcn_exp2f((s - M) * 1.4426950408889634f);
    Lf[L_P + w * 64 + lane] = pe;
    const float lw = wave_sum(pe);
    if (lane == 0) Lf[L_L + w] = lw;
    const unsigned ord = lds_arrive(L + L_FLAG + F_P, lane);
    if (ord == 7u) {
      const v4f l0 = *(LAS v4f*)(Lf + L_L), l1 = *(LAS v4f*)(Lf + L_L + 4);
      const float Ls = ((l0[0] + l0[1]) + (l0[2] + l0[3])) + ((l1[0] + l1[1]) + (l1[2] + l1[3]));
      if (lane == 0) st_gran2(rws, ML_OFF + hd * 128 + sp * 16, M, Ls, T);
    }
    if (w < 5) {
      lds_wait_ge(L + L_FLAG + F_VISS, 8u);
      STAMP(4);
      const int g = lane >> 4, c = lane & 15, half = c >> 3;
      const rsrc_t rwo = mk_rsrc(p.wo + (size_t)(hd * DH + sp * 24) * D, 24 * D * 2);
      v4u wr[6];
#pragma unroll
      for (int i = 0; i < 6; ++i) {
        const int row = 6 * g + 3 * (half ^ (i / 3)) + (i % 3);
        wr[i] = __builtin_amdgcn_raw_buffer_load_b128(rwo, row * (D * 2) + (128 * w + 8 * c) * 2, 0, AUX_NT);
      }
      __builtin_amdgcn_sched_barrier(0);
      const int col = 128 * w + 8 * c + 4 * (g & 1) + 2 * (g >> 1);
      lds_wait_ge(L + L_FLAG + F_SCALE, 1u);
      const float wsp = Lf[L_SC + (c & 7)];
      float y0 = 0.f, y1 = 0.f;
      if (has_ffn) {
        lds_wait_ge(L + L_FLAG + F_B, 1u);
        const int rel = col - 64 * fk;
        const float omg = Lf[L_SC + 8];
        if (rel >= 0 && rel < 64) { y0 = omg * Lf[L_BB + rel]; y1 = omg * Lf[L_BB + rel + 1]; }
      }
      lds_wait_ge(L + L_FLAG + F_OPUB, 1u);
      STAMP(5);
      v4u og[1];
      gather<1, false, 2>(rws, OP_OFF + (((hd * 8 + (c & 7)) * 8 + sp) * 8 + 2 * g + half) * 16, 0, T, og);
      STAMP(6);
      float o6[6];
#pragma unroll
      for (int j = 0; j < 3; ++j) {
        float t = wsp * __uint_as_float(og[0][j]);
        t += dpp<XOR1>(t); t += dpp<XOR2>(t); t += dpp<XOR7>(t);
        o6[j] = t; o6[3 + j] = dpp<XOR8>(t);
      }
      float acc[8] = {0.f, 0.f, 0.f, 0.f, 0.f, 0.f, 0.f, 0.f};
#pragma unroll
      for (int i = 0; i < 6; ++i) fma8(acc, o6[i], wr[i]);
      float z[4];
#pragma unroll
      for (int j = 0; j < 4; ++j) z[j] = swap16_add(acc[j], acc[j + 4]);
      y0 += swap32_add(z[0], z[2]); y1 += swap32_add(z[1], z[3]);
      st_gran2(rws, YP_OFF + (b * 640 + col) * 8, y0, y1, T);
      STAMP(7);
      if (b < NRED) lds_arrive(L + L_FLAG + F_YPUB, lane);
    } else if (w == 5) {
      if (has_ffn) {
        const rsrc_t rwd = mk_rsrc(p.wd + (size_t)(64 * fc) * D + 64 * fk, (63 * D + 64) * 2);
        v4u wr[8];
#pragma unroll
        for (int i = 0; i < 8; ++i) wr[i] = __builtin_amdgcn_raw_buffer_load_b128(rwd, (lane >> 3) * (D * 2) + (lane & 7) * 16, i * 8 * D * 2, AUX_NT);
        float a0 = 0.f, a1 = 0.f;
        {
          v4u gr[10];
          gather<10, true, 8>(rws, GP_OFF + fc * 10 * 1024 + lane * 16, 1024, T, gr);
#pragma unroll
          for (int k = 0; k < 10; ++k) { a0 += __uint_as_float(gr[k][0]); a1 += __uint_as_float(gr[k][2]); }
        }
        a0 = rbf(a0); a1 = rbf(a1);
        auto s0 = __builtin_amdgcn_permlane32_swap(__float_as_uint(a0), __float_as_uint(a0), false, false);
        auto s1 = __builtin_amdgcn_permlane32_swap(__float_as_uint(a1), __float_as_uint(a1), false, false);
        const float g0 = __uint_as_float(s0[0]), u0 = __uint_as_float(s0[1]), g1 = __uint_as_float(s1[0]), u1 = __uint_as_float(s1[1]);
        const unsigned hp = bfbits((g0 > 0.f ? g0 : 0.f) * u0) | (bfbits((g1 > 0.f ? g1 : 0.f) * u1) << 16);
        float r0, r1;
        gemv_rows<8>(wr, hp, lane, r0, r1);
        if (!(lane & 8)) { Lf[L_BB + cc2] = r0; Lf[L_BB + cc2 + 1] = r1; }
        lds_arrive(L + L_FLAG + F_B, lane);
        STAMP(4);
      }
    } else if (w == 6) {
      float z = 0.f;
#pragma unroll
      for (int i = 0; i < 5; ++i) {
        const unsigned xv = ((const GAS unsigned*)p.x)[lane + 64 * i], gv = ((const GAS unsigned*)p.gw)[lane + 64 * i];
        z += rbf(bf_lo(xv) * bf_lo(gv)); z += rbf(bf_hi(xv) * bf_hi(gv));
      }
      z = rbf(wave_sum(z));
      const float gate = rbf(1.f / (1.f + __expf(-z)));
      const float omg = rbf(1.f - gate);
      v4u mg[1];
      gather<1, true, 8>(rws, ML_OFF + hd * 128 + (lane & 7) * 16, 0, T, mg);
      const float ms = __uint_as_float(mg[0][0]), ls = __uint_as_float(mg[0][2]);
      float Mh = ms;
      Mh = fmaxf(Mh, dpp<XOR1>(Mh)); Mh = fmaxf(Mh, dpp<XOR2>(Mh)); Mh = fmaxf(Mh, dpp<XOR7>(Mh));
      const float es = __builtin_amdgcn_exp2f((ms - Mh) * 1.4426950408889634f);
      float Lh = ls * es;
      Lh += dpp<XOR1>(Lh); Lh += dpp<XOR2>(Lh); Lh += dpp<XOR7>(Lh);
      if (lane < 8) Lf[L_SC + lane] = es / Lh * gate;
      if (lane == 0) Lf[L_SC + 8] = omg;
      lds_arrive(L + L_FLAG + F_SCALE, lane);
      STAMP(4);
    }
  } else {
    const int v = w - 8;
    WG_BARRIER2();
    STAMP(1);
    const rsrc_t rv = mk_rsrc(p.vc + kv_off, 64 * DH * 2);
    const int voff = (lane >> 3) * 384 + (lane & 7) * 16;
    v4u vr[24];
#pragma unroll
    for (int t = 0; t < 3; ++t)
#pragma unroll
      for (int g = 0; g < 8; ++g) {
        vr[g * 3 + t] = __builtin_amdgcn_raw_buffer_load_b128(rv, voff + t * 128, g * 3072, AUX_NT);
        __builtin_amdgcn_sched_barrier(0);
      }
    const unsigned T = GET_TAG();
    STAMP(2);
    lds_arrive(L + L_FLAG + F_VISS, lane);
    lds_wait_ge(L + L_FLAG + F_P, 8u);
    STAMP(3);
    {
      const v4f pa = *(LAS v4f*)(Lf + L_P + v * 64 + (lane & ~7)), pb = *(LAS v4f*)(Lf + L_P + v * 64 + (lane & ~7) + 4);
#pragma unroll
      for (int t = 0; t < 3; ++t) {
        float acc[8] = {0.f, 0.f, 0.f, 0.f, 0.f, 0.f, 0.f, 0.f};
#pragma unroll
        for (int g = 0; g < 8; ++g) fma8(acc, g < 4 ? pa[g & 3] : pb[g & 3], vr[g * 3 + t]);
        float r0, r1;
        red_rows8(acc, r0, r1);
        if (!(lane & 8)) { Lf[L_O + v * 192 + 64 * t + cc2] = r0; Lf[L_O + v * 192 + 64 * t + cc2 + 1] = r1; }
        __builtin_amdgcn_sched_barrier(0);
      }
    }
    STAMP(4);
    const unsigned ord = lds_arrive(L + L_FLAG + F_O, lane);
    if (ord == 7u) {
      float s0 = 0.f, s1 = 0.f, s2 = 0.f;
#pragma unroll
      for (int u = 0; u < 8; ++u) { s0 += Lf[L_O + u * 192 + 3 * lane]; s1 += Lf[L_O + u * 192 + 3 * lane + 1]; s2 += Lf[L_O + u * 192 + 3 * lane + 2]; }
      const v4u g3 = {__float_as_uint(s0), __float_as_uint(s1), __float_as_uint(s2), T};
      __builtin_amdgcn_raw_buffer_store_b128(g3, rws, OP_OFF + ((hd * 8 + sp) * 64 + lane) * 16, 0, AUX_SC1);
      lds_arrive(L + L_FLAG + F_OPUB, lane);
      STAMP(5);
    }
    if (b < NRED) {
      lds_wait_ge(L + L_FLAG + F_YPUB, 1u);
      float a0 = 0.f, a1 = 0.f;
      {
        v4u gr[4];
        gather<4, true, 2>(rws, YP_OFF + ((32 * v + (lane >> 3)) * 640 + 16 * b + 2 * (lane & 7)) * 8, 8 * 640 * 8, T, gr);
#pragma unroll
        for (int i = 0; i < 4; ++i) { a0 += __uint_as_float(gr[i][0]); a1 += __uint_as_float(gr[i][2]); }
      }
      STAMP(6);
      a0 += dpp<XOR8>(a0); a1 += dpp<XOR8>(a1);
      a0 = swap16_add(a0, a0); a1 = swap16_add(a1, a1);
      a0 = swap32_add(a0, a0); a1 = swap32_add(a1, a1);
      if (lane < 8) { Lf[L_FIN + v * 16 + 2 * lane] = a0; Lf[L_FIN + v * 16 + 2 * lane + 1] = a1; }
      const unsigned o2 = lds_arrive(L + L_FLAG + F_FIN, lane);
      if (o2 == 7u) {
        if (lane < 8) {
          float s0 = 0.f, s1 = 0.f;
#pragma unroll
          for (int u = 0; u < 8; ++u) { s0 += Lf[L_FIN + u * 16 + 2 * lane]; s1 += Lf[L_FIN + u * 16 + 2 * lane + 1]; }
          ((GAS unsigned*)p.out)[8 * b + lane] = bfbits(s0) | (bfbits(s1) << 16);
        }
        if (b == 0 && lane == 0) *(GAS unsigned*)(p.ws + E_OFF) = T;
        STAMP(7);
      }
    }
  }
  STAMP_FLUSH();
}

__global__ void __launch_bounds__(1024) k_leak(unsigned char* ws) {
  __shared__ unsigned long long smn[1024], smx[1024];
  const unsigned long long tstart = __builtin_amdgcn_s_memrealtime();
  const unsigned long long* hs = (const unsigned long long*)(ws + STAMP_OFF);
  unsigned long long mn = ~0ull, mx = 0;
  for (int i = threadIdx.x; i < 256 * 16; i += 1024) { unsigned long long t = hs[i * NSTAMP]; mn = t < mn ? t : mn; }
  smn[threadIdx.x] = mn; __syncthreads();
  for (int o = 512; o > 0; o >>= 1) { if (threadIdx.x < o) { unsigned long long a = smn[threadIdx.x], b = smn[threadIdx.x + o]; smn[threadIdx.x] = a < b ? a : b; } __syncthreads(); }
  mn = smn[0];
  for (int i = threadIdx.x; i < 256 * 16; i += 1024) { const int w = i & 15; if (!(LEAK_SEL)) continue; unsigned long long t = hs[i * NSTAMP + LEAK_K]; if (t >= mn && t < mn + 100000) mx = t > mx ? t : mx; }
  smx[threadIdx.x] = mx; __syncthreads();
  for (int o = 512; o > 0; o >>= 1) { if (threadIdx.x < o) { unsigned long long a = smx[threadIdx.x], b = smx[threadIdx.x + o]; smx[threadIdx.x] = a > b ? a : b; } __syncthreads(); }
  mx = smx[0];
  const unsigned long long d = mx - mn;
  const unsigned long long until = tstart + 1000 + 10 * (d < 10000 ? d : 10000);
  while (__builtin_amdgcn_s_memrealtime() < until) __builtin_amdgcn_s_sleep(4);
}
extern "C" void kernel_launch(void* const* d_in, const int* in_sizes, int n_in,
                              void* d_out, int out_size, void* d_ws, size_t ws_size,
                              hipStream_t stream) {
  Params p;
  p.x = (const unsigned short*)d_in[0];
  p.kc = (const unsigned short*)d_in[1];
  p.vc = (const unsigned short*)d_in[2];
  p.wq = (const unsigned short*)d_in[3];
  p.wo = (const unsigned short*)d_in[4];
  p.wg = (const unsigned short*)d_in[6];
  p.wu = (const unsigned short*)d_in[7];
  p.wd = (const unsigned short*)d_in[8];
  p.gw = (const unsigned short*)d_in[9];
  p.out = (unsigned short*)d_out;
  p.ws = (unsigned char*)d_ws;
  fused_decode<<<NWG, NTHR, 0, stream>>>(p);
  k_leak<<<1, 1024, 0, stream>>>(p.ws);
}
```

```cpp
#include <hip/hip_runtime.h>
#include <hip/hip_bf16.h>
#include <stdint.h>

#define GAS __attribute__((address_space(1)))
#define LAS __attribute__((address_space(3)))
typedef unsigned v4u __attribute__((ext_vector_type(4)));
typedef float v4f __attribute__((ext_vector_type(4)));
typedef __bf16 bf16x2 __attribute__((ext_vector_type(2)));
typedef __amdgpu_buffer_rsrc_t rsrc_t;

#define LSLEEP 1
#define QDELAY 40
#define WO_EARLY 0
#define POLL_LATE 1
#define WO_SLACK 1
#define WO_DELTA 0
#define PSLEEP 1
#define MLSLEEP 2
#define LOOKAHEAD 7

constexpr int D = 640, HQ = 32, DH = 192, SK = 32768, WIN = 4096, FF = 1536;
constexpr int NWG = 256, NTHR = 1024;
constexpr int NRED = 40;
constexpr int UK = 32;
#define NSTATIC 13
constexpr int NS = NSTATIC;
constexpr int NP = WIN / UK - 8 * NS;
constexpr int NPART = 8 + NP;
static_assert(NPART % 8 == 0, "gather shape");
constexpr int E_OFF = 0;
constexpr int CNT_OFF = 4096;
constexpr int QP_OFF = CNT_OFF + 32 * 128;
constexpr int GP_OFF = QP_OFF + 32 * 3072;
constexpr int ML_OFF = GP_OFF + 24 * 2048;
constexpr int OP_OFF = ML_OFF + 32 * 1024;
constexpr int YP_OFF = OP_OFF + 32 * NPART * 1024;
constexpr int WS_BYTES = YP_OFF + 128 * 640 * 8;
constexpr int AUX_NT = 2, AUX_SC1 = 16;
constexpr unsigned SPIN_CAP = 1u << 20;
constexpr float LOG2E = 1.4426950408889634f;
constexpr int NG = NPART / 8;

constexpr int L_FLAG = 0;
constexpr int F_Q = 0, F_ISS = 1, F_ISS_S = 2, F_TICKET = 3, F_EMPTY = 4, F_SARR = 5, F_G = 6, F_B = 7, F_FIN = 8, F_YPUB = 9, F_SML = 10, F_W = 11,
              F_CLAIMED = 12, F_SLOT = 13, F_DONE = 14, F_WT = 15, F_P1 = 16, F_TE = 18;
constexpr int L_Q = 32;
constexpr int L_M = 128;
constexpr int L_L = 144;
constexpr int L_G = 160;
constexpr int L_BB = 164;
constexpr int L_FIN = 228;
constexpr int L_WT = 356;
constexpr int L_GS = 396;
constexpr int L_P1 = L_GS + 5 * 64;
constexpr int L_O = L_P1 + 512;
constexpr int L_TOTAL = L_O + NS * 192;

__device__ __forceinline__ rsrc_t mk_rsrc(const void* p, int bytes) {
  return __builtin_amdgcn_make_buffer_rsrc((void*)p, 0, bytes, 0x00020000);
}
__device__ __forceinline__ float bf_lo(unsigned u) { return __uint_as_float(u << 16); }
__device__ __forceinline__ float bf_hi(unsigned u) { return __uint_as_float(u & 0xffff0000u); }
__device__ __forceinline__ float rbf(float f) { return __bfloat162float(__float2bfloat16(f)); }
__device__ __forceinline__ unsigned bfbits(float f) { return (unsigned)__builtin_bit_cast(unsigned short, __float2bfloat16(f)); }
__device__ __forceinline__ float dot2(unsigned a, unsigned b, float c) {
  return __builtin_amdgcn_fdot2_f32_bf16(__builtin_bit_cast(bf16x2, a), __builtin_bit_cast(bf16x2, b), c, false);
}
template <int CTRL> __device__ __forceinline__ float dpp(float x) {
  return __builtin_bit_cast(float, __builtin_amdgcn_mov_dpp(__builtin_bit_cast(int, x), CTRL, 0xf, 0xf, true));
}
constexpr int XOR1 = 0xB1, XOR2 = 0x4E, XOR7 = 0x141, XOR8 = 0x128;
__device__ __forceinline__ float swap16_add(float a, float b) {
  auto s = __builtin_amdgcn_permlane16_swap(__float_as_uint(a), __float_as_uint(b), false, false);
  return __uint_as_float(s[0]) + __uint_as_float(s[1]);
}
__device__ __forceinline__ float swap32_add(float a, float b) {
  auto s = __builtin_amdgcn_permlane32_swap(__float_as_uint(a), __float_as_uint(b), false, false);
  return __uint_as_float(s[0]) + __uint_as_float(s[1]);
}
__device__ __forceinline__ float swap16_max(float a) {
  auto s = __builtin_amdgcn_permlane16_swap(__float_as_uint(a), __float_as_uint(a), false, false);
  return fmaxf(__uint_as_float(s[0]), __uint_as_float(s[1]));
}
__device__ __forceinline__ float swap32_max(float a) {
  auto s = __builtin_amdgcn_permlane32_swap(__float_as_uint(a), __float_as_uint(a), false, false);
  return fmaxf(__uint_as_float(s[0]), __uint_as_float(s[1]));
}
__device__ __forceinline__ float sum8(float x) { x += dpp<XOR1>(x); x += dpp<XOR2>(x); x += dpp<XOR7>(x); return x; }
__device__ __forceinline__ float max8(float x) { x = fmaxf(x, dpp<XOR1>(x)); x = fmaxf(x, dpp<XOR2>(x)); return fmaxf(x, dpp<XOR7>(x)); }
__device__ __forceinline__ float sum_rows(float x) { x += dpp<XOR8>(x); x = swap16_add(x, x); return swap32_add(x, x); }
__device__ __forceinline__ float max_rows(float x) { x = fmaxf(x, dpp<XOR8>(x)); x = swap16_max(x); return swap32_max(x); }
__device__ __forceinline__ void red_rows8(const float (&a)[8], float& r0, float& r1) {
  float z[4];
#pragma unroll
  for (int j = 0; j < 4; ++j) z[j] = swap16_add(a[j], a[j + 4]);
  r0 = swap32_add(z[0], z[2]); r1 = swap32_add(z[1], z[3]);
  r0 += dpp<XOR8>(r0); r1 += dpp<XOR8>(r1);
}
__device__ __forceinline__ void fma8(float (&acc)[8], float s, v4u w) {
#pragma unroll
  for (int j = 0; j < 4; ++j) { acc[2 * j] = fmaf(s, bf_lo(w[j]), acc[2 * j]); acc[2 * j + 1] = fmaf(s, bf_hi(w[j]), acc[2 * j + 1]); }
}
#define WG_BARRIER() do { __builtin_amdgcn_sched_barrier(0); asm volatile("s_barrier" ::: "memory"); __builtin_amdgcn_sched_barrier(0); } while (0)
#define PRIO_HI() __builtin_amdgcn_s_setprio(3)
#define PRIO_LO() do { __builtin_amdgcn_sched_barrier(0); __builtin_amdgcn_s_setprio(0); } while (0)
#define CBAR() asm volatile("" ::: "memory")
#define LGKM0() asm volatile("s_waitcnt lgkmcnt(0)" ::: "memory")
__device__ __forceinline__ unsigned lds_ld(LAS unsigned* p) { return __hip_atomic_load(p, __ATOMIC_RELAXED, __HIP_MEMORY_SCOPE_WORKGROUP); }
__device__ __forceinline__ void lds_wait_ge(LAS unsigned* p, unsigned want) {
#pragma unroll 1
  for (unsigned s = 0; s < SPIN_CAP; ++s) {
    CBAR();
    if ((unsigned)__builtin_amdgcn_readfirstlane(lds_ld(p)) >= want) break;
    __builtin_amdgcn_s_sleep(LSLEEP);
  }
  CBAR();
}
__device__ __forceinline__ unsigned lds_arrive(LAS unsigned* p, int lane) {
  LGKM0();
  unsigned old = 0;
  if (lane == 0) old = __hip_atomic_fetch_add(p, 1u, __ATOMIC_RELAXED, __HIP_MEMORY_SCOPE_WORKGROUP);
  old = __builtin_amdgcn_readfirstlane(old);
  CBAR();
  return old;
}
__device__ __forceinline__ void wait_all_issued(LAS unsigned* L) {
#pragma unroll 1
  for (unsigned s = 0; s < SPIN_CAP; ++s) {
    CBAR();
    const unsigned e = __builtin_amdgcn_readfirstlane(lds_ld(L + L_FLAG + F_EMPTY));
    const unsigned c = __builtin_amdgcn_readfirstlane(lds_ld(L + L_FLAG + F_CLAIMED));
    const unsigned n = __builtin_amdgcn_readfirstlane(lds_ld(L + L_FLAG + F_ISS));
    if (e && n + WO_EARLY >= 4 * (NS + c)) break;
    __builtin_amdgcn_s_sleep(LSLEEP);
  }
  CBAR();
}
__device__ __forceinline__ void wait_done(LAS unsigned* L, unsigned slack) {
#pragma unroll 1
  for (unsigned s = 0; s < SPIN_CAP; ++s) {
    CBAR();
    const unsigned e = __builtin_amdgcn_readfirstlane(lds_ld(L + L_FLAG + F_EMPTY));
    const unsigned c = __builtin_amdgcn_readfirstlane(lds_ld(L + L_FLAG + F_CLAIMED));
    const unsigned n = __builtin_amdgcn_readfirstlane(lds_ld(L + L_FLAG + F_DONE));
    if (e && n + slack >= 1u + c) break;
    __builtin_amdgcn_s_sleep(LSLEEP);
  }
  CBAR();
}
__device__ __forceinline__ void st_gran2(rsrc_t rs, int off, float a, float b, unsigned T) {
  v4u g = {__float_as_uint(a), T, __float_as_uint(b), T};
  __builtin_amdgcn_raw_buffer_store_b128(g, rs, off, 0, AUX_SC1);
}
__device__ __forceinline__ void st_gran3(rsrc_t rs, int off, float a, float b, float c, unsigned T) {
  v4u g = {__float_as_uint(a), __float_as_uint(b), __float_as_uint(c), T};
  __builtin_amdgcn_raw_buffer_store_b128(g, rs, off, 0, AUX_SC1);
}
template <int N, bool PAIR, int SLEEP>
__device__ __forceinline__ void gather(rsrc_t rs, int off, int stride, unsigned T, v4u (&gr)[N]) {
  CBAR();
#pragma unroll
  for (int i = 0; i < N; ++i) gr[i] = __builtin_amdgcn_raw_buffer_load_b128(rs, off + i * stride, 0, AUX_SC1);
#pragma unroll 1
  for (unsigned s2 = 0; s2 < SPIN_CAP; ++s2) {
    unsigned bad = 0u;
#pragma unroll
    for (int i = 0; i < N; ++i) bad |= PAIR ? ((gr[i][1] ^ T) | (gr[i][3] ^ T)) : (gr[i][3] ^ T);
    if (__all(bad == 0u)) break;
    __builtin_amdgcn_s_sleep(SLEEP);
    CBAR();
#pragma unroll
    for (int i = 0; i < N; ++i) {
      const bool have = PAIR ? (((gr[i][1] ^ T) | (gr[i][3] ^ T)) == 0u) : (gr[i][3] == T);
      if (!have) gr[i] = __builtin_amdgcn_raw_buffer_load_b128(rs, off + i * stride, 0, AUX_SC1);
    }
  }
  CBAR();
}
template <bool PAIR>
__device__ __forceinline__ v4u poll1(rsrc_t rs, int off, unsigned T) {
  CBAR();
  v4u g = __builtin_amdgcn_raw_buffer_load_b128(rs, off, 0, AUX_SC1);
#pragma unroll 1
  for (unsigned s2 = 0; s2 < SPIN_CAP; ++s2) {
    const bool miss = PAIR ? (((g[1] ^ T) | (g[3] ^ T)) != 0u) : (g[3] != T);
    if (!__any(miss)) break;
    __builtin_amdgcn_s_sleep(PSLEEP);
    CBAR();
    if (miss) g = __builtin_amdgcn_raw_buffer_load_b128(rs, off, 0, AUX_SC1);
  }
  CBAR();
  return g;
}
template <int NI>
__device__ __forceinline__ void gemv_rows(const v4u (&wr)[NI], unsigned xv, int lane, float& r0, float& r1) {
  float acc[8] = {0.f, 0.f, 0.f, 0.f, 0.f, 0.f, 0.f, 0.f};
  const int r = lane >> 3;
#pragma unroll
  for (int i = 0; i < NI; ++i) {
    const unsigned hv = (unsigned)__builtin_amdgcn_ds_bpermute((4 * i + (r >> 1)) * 4, (int)xv);
    fma8(acc, (r & 1) ? bf_hi(hv) : bf_lo(hv), wr[i]);
  }
  red_rows8(acc, r0, r1);
}
#define STAMP(k) do {} while (0)
#define STAMP_FLUSH() do {} while (0)
struct Params {
  const unsigned short *x, *kc, *vc, *wq, *wo, *wg, *wu, *wd, *gw;
  unsigned short* out;
  unsigned char* ws;
};

__global__ void __launch_bounds__(NTHR, 4) fused_decode(Params p) {
  __shared__ __attribute__((aligned(16))) unsigned lds_raw[L_TOTAL];
  LAS unsigned* L = (LAS unsigned*)lds_raw;
  LAS float* Lf = (LAS float*)lds_raw;
  const int b = blockIdx.x, tid = threadIdx.x, lane = tid & 63;
  const int w = __builtin_amdgcn_readfirstlane(tid >> 6);
  STAMP(0);
  if (tid < 32) L[L_FLAG + tid] = 0u;
#define WO_WAIT() wait_done(L, WO_SLACK)
  LGKM0();
  const int hd = b >> 3, sp = b & 7;
  const rsrc_t rws = mk_rsrc(p.ws, WS_BYTES);
  const unsigned Eraw = *(const __attribute__((address_space(4))) unsigned*)(p.ws + E_OFF);
#define GET_TAG() ({ unsigned t_; asm volatile("s_add_u32 %0, %1, 1" : "=s"(t_) : "s"(Eraw) : "scc"); t_; })
  const int cc2 = 8 * (lane & 7) + 4 * ((lane >> 4) & 1) + 2 * (lane >> 5);
  const int rc_off = (lane & 7) * 16;
  unsigned T = 0u;

  if (w >= 4) WG_BARRIER();
  if (w < 4) {
    const int nround = (sp == 6 && w >= 2) ? 3 : 1;
#pragma unroll 1
    for (int round = 0; round < nround; ++round) {
      const bool p1a = round == 0;
      const int wv = p1a ? w : 2 * (w - 2) + round - 1;
      const bool isq = p1a && sp < 6;
      const int f = p1a ? hd * 2 + (sp - 6) : 64 + hd;
      const int half = isq ? (sp & 1) : (f & 1);
      const int row0 = 320 * half + 80 * wv;
      const int ld = isq ? HQ * DH : FF;
      const unsigned short* base = isq ? p.wq + hd * DH + 64 * (sp >> 1) : ((f & 2) ? p.wu : p.wg) + 64 * (f >> 2);
      const int pub = isq ? QP_OFF + (hd * 6 + sp) * 512 : GP_OFF + f * 512;
      if (w == 0 && sp == 0 && lane == 0) __builtin_amdgcn_raw_buffer_store_b32(0u, rws, CNT_OFF + hd * 128, 0, AUX_SC1);
      const rsrc_t rq = mk_rsrc(base + (size_t)row0 * ld, (79 * ld + 64) * 2);
      const unsigned xq = ((const GAS unsigned*)p.x)[(row0 >> 1) + (lane < 40 ? lane : 39)];
      v4u wr[10];
#pragma unroll
      for (int i = 0; i < 10; ++i) wr[i] = __builtin_amdgcn_raw_buffer_load_b128(rq, (lane >> 3) * (ld * 2) + rc_off, i * 16 * ld, AUX_NT);
      if (p1a) WG_BARRIER();
      float r0, r1;
      gemv_rows<10>(wr, xq, lane, r0, r1);
      if (p1a) T = GET_TAG();
      asm volatile("s_waitcnt vmcnt(0)" ::: "memory");
      LAS float* slot = Lf + L_P1 + (p1a ? 0 : 256);
      if (!(lane & 8)) { slot[wv * 64 + cc2] = r0; slot[wv * 64 + cc2 + 1] = r1; }
      if (lds_arrive(L + L_FLAG + F_P1 + (p1a ? 0 : 1), lane) == 3u && lane < 32) {
        const float s0 = (slot[2 * lane] + slot[64 + 2 * lane]) + (slot[128 + 2 * lane] + slot[192 + 2 * lane]);
        const float s1 = (slot[2 * lane + 1] + slot[65 + 2 * lane]) + (slot[129 + 2 * lane] + slot[193 + 2 * lane]);
        st_gran2(rws, pub + lane * 16, s0, s1, T);
      }
    }
  }
  const int fc = (b >> 1) / 5, fk = 5 * (b & 1) + (b >> 1) % 5;
  const bool has_ffn = b < 240;
  if (w == 0) {
    STAMP(1);
    __builtin_amdgcn_s_sleep(QDELAY);
    {
      v4u gr[3];
      gather<3, true, 8>(rws, QP_OFF + hd * 3072 + lane * 16, 1024, T, gr);
#pragma unroll
      for (int c = 0; c < 3; ++c) {
        const float q0 = swap32_add(__uint_as_float(gr[c][0]), __uint_as_float(gr[c][0])), q1 = swap32_add(__uint_as_float(gr[c][2]), __uint_as_float(gr[c][2]));
        if (lane < 32) L[L_Q + 32 * c + lane] = bfbits(q0) | (bfbits(q1) << 16);
      }
    }
    lds_arrive(L + L_FLAG + F_Q, lane);
    STAMP(2);
    float z = 0.f;
#pragma unroll
    for (int i = 0; i < 5; ++i) {
      const unsigned xv = ((const GAS unsigned*)p.x)[lane + 64 * i], gv = ((const GAS unsigned*)p.gw)[lane + 64 * i];
      z += rbf(bf_lo(xv) * bf_lo(gv)); z += rbf(bf_hi(xv) * bf_hi(gv));
    }
    z = rbf(sum_rows(sum8(z)));
    const float gate = rbf(1.f / (1.f + __expf(-z)));
    if (lane == 0) { Lf[L_G] = gate; Lf[L_G + 1] = rbf(1.f - gate); }
    lds_arrive(L + L_FLAG + F_G, lane);
    STAMP(3);
  } else if (w == 3 && has_ffn) {
    lds_wait_ge(L + L_FLAG + F_ISS, 4 * NS - 4);
    unsigned hp;
    {
      v4u gr[2];
      gather<2, true, 16>(rws, GP_OFF + fc * 2048 + lane * 16, 1024, T, gr);
      const float g0 = rbf(swap32_add(__uint_as_float(gr[0][0]), __uint_as_float(gr[0][0]))), g1 = rbf(swap32_add(__uint_as_float(gr[0][2]), __uint_as_float(gr[0][2])));
      const float u0 = rbf(swap32_add(__uint_as_float(gr[1][0]), __uint_as_float(gr[1][0]))), u1 = rbf(swap32_add(__uint_as_float(gr[1][2]), __uint_as_float(gr[1][2])));
      hp = bfbits((g0 > 0.f ? g0 : 0.f) * u0) | (bfbits((g1 > 0.f ? g1 : 0.f) * u1) << 16);
    }
    STAMP(2);
    wait_all_issued(L);
    WO_WAIT();
    const rsrc_t rwd = mk_rsrc(p.wd + (size_t)(64 * fc) * D + 64 * fk, (63 * D + 64) * 2);
    v4u wr[8];
#pragma unroll
    for (int i = 0; i < 8; ++i) wr[i] = __builtin_amdgcn_raw_buffer_load_b128(rwd, (lane >> 3) * (D * 2) + rc_off, i * 8 * D * 2, AUX_NT);
    __builtin_amdgcn_sched_barrier(0);
    float r2, r3;
    gemv_rows<8>(wr, hp, lane, r2, r3);
    lds_wait_ge(L + L_FLAG + F_G, 1u);
    const float omg = Lf[L_G + 1];
    if (!(lane & 8)) { Lf[L_BB + cc2] = omg * r2; Lf[L_BB + cc2 + 1] = omg * r3; }
    lds_arrive(L + L_FLAG + F_B, lane);
    STAMP(3);
  }

  int su = w >= 4 ? w - 4 : ((w == 1 || w == 2) && 11 + w < NS ? 11 + w : -1);
  const int koff = (lane >> 3) * 384 + rc_off;
  const int dgr = cc2 + ((lane >> 3) & 1);
  const bool no_units = w == 3;
#pragma unroll 1
  for (; !no_units;) {
    int key0, part;
    if (su >= 0) {
      key0 = sp * (NS * UK) + su * UK; part = -1;
    } else {
      const unsigned tk = lds_arrive(L + L_FLAG + F_TICKET, lane);
      bool empty = false;
#pragma unroll 1
      for (unsigned s = 0; s < SPIN_CAP; ++s) {
        CBAR();
        const unsigned e = __builtin_amdgcn_readfirstlane(lds_ld(L + L_FLAG + F_EMPTY));
        const unsigned n = __builtin_amdgcn_readfirstlane(lds_ld(L + L_FLAG + F_ISS));
        const unsigned q = __builtin_amdgcn_readfirstlane(lds_ld(L + L_FLAG + F_Q));
        if (e) { empty = true; break; }
        if (q && n + LOOKAHEAD >= 4 * (NS + tk)) break;
        __builtin_amdgcn_s_sleep(LSLEEP);
      }
      if (empty) break;
      unsigned u = 0;
      if (lane == 0) u = __hip_atomic_fetch_add((GAS unsigned*)(p.ws + CNT_OFF + hd * 128), 1u, __ATOMIC_RELAXED, __HIP_MEMORY_SCOPE_AGENT);
      u = __builtin_amdgcn_readfirstlane(u);
      if (u >= (unsigned)NP) {
        lds_arrive(L + L_FLAG + F_EMPTY, lane); break; }
      key0 = 8 * NS * UK + (int)u * UK; part = 8 + (int)u;
      lds_arrive(L + L_FLAG + F_CLAIMED, lane);
      lds_wait_ge(L + L_FLAG + F_ISS_S, NS);
      STAMP(4);
    }
    const size_t kv_off = ((size_t)hd * SK + (SK - WIN) + key0) * DH;
    const rsrc_t rk = mk_rsrc(p.kc + kv_off, UK * DH * 2);
    const rsrc_t rv = mk_rsrc(p.vc + kv_off, UK * DH * 2);
    v4u kr[12], vr[12];
#define TICK() do { if (lane == 0) __hip_atomic_fetch_add(L + L_FLAG + F_ISS, 1u, __ATOMIC_RELAXED, __HIP_MEMORY_SCOPE_WORKGROUP); __builtin_amdgcn_sched_barrier(0); } while (0)
#pragma unroll
    for (int g = 0; g < 4; ++g) {
#pragma unroll
      for (int t = 0; t < 3; ++t) { kr[g * 3 + t] = __builtin_amdgcn_raw_buffer_load_b128(rk, koff + t * 128, g * 3072, AUX_NT); __builtin_amdgcn_sched_barrier(0); }
      if (g & 1) TICK();
    }
#pragma unroll
    for (int i = 0; i < 12; ++i) {
      const int t = i >> 2, g = i & 3;
      vr[g * 3 + t] = __builtin_amdgcn_raw_buffer_load_b128(rv, koff + t * 128, g * 3072, AUX_NT); __builtin_amdgcn_sched_barrier(0);
      if (i == 5 || i == 11) TICK();
    }
    if (part < 0) { if (w >= 4) T = GET_TAG(); lds_arrive(L + L_FLAG + F_ISS_S, lane); STAMP(1); }
    lds_wait_ge(L + L_FLAG + F_Q, 1u);
    float pe[4], mu, lu;
    {
      v4u qf[3];
#pragma unroll
      for (int t = 0; t < 3; ++t) qf[t] = *(LAS v4u*)(L + L_Q + 32 * t + 4 * (lane & 7));
      float s[4];
#pragma unroll
      for (int g = 0; g < 4; ++g) {
        float a = 0.f;
#pragma unroll
        for (int t = 0; t < 3; ++t)
#pragma unroll
          for (int j = 0; j < 4; ++j) a = dot2(kr[g * 3 + t][j], qf[t][j], a);
        s[g] = rbf(rbf(sum8(a)) * 0.07216878364870322f);
      }
      mu = max_rows(fmaxf(fmaxf(s[0], s[1]), fmaxf(s[2], s[3])));
#pragma unroll
      for (int g = 0; g < 4; ++g) pe[g] = __builtin_amdgcn_exp2f((s[g] - mu) * LOG2E);
      lu = sum_rows((pe[0] + pe[1]) + (pe[2] + pe[3]));
    }
    PRIO_HI();
    {
      float pm = mu, pl = lu;
      int slot = part;
      if (part < 0) {
        if (lane == 0) { Lf[L_M + su] = mu; Lf[L_L + su] = lu; }
        if (lds_arrive(L + L_FLAG + F_SML, lane) == NS - 1) {
          const float mj = Lf[L_M + (lane < NS ? lane : 0)], lj = Lf[L_L + (lane < NS ? lane : 0)];
          pm = max_rows(max8(mj));
          pl = sum_rows(sum8(lane < NS ? __builtin_amdgcn_exp2f((mj - pm) * LOG2E) * lj : 0.f));
          slot = sp;
        }
      }
      st_gran2(rws, (slot >= 0 && lane == 0) ? ML_OFF + hd * 1024 + slot * 16 : 0x7ffffff0, pm, pl, T);
      __builtin_amdgcn_sched_barrier(0);
    }
    float o3[3];
#pragma unroll
    for (int t = 0; t < 3; ++t) {
      float acc[8] = {0.f, 0.f, 0.f, 0.f, 0.f, 0.f, 0.f, 0.f};
#pragma unroll
      for (int g = 0; g < 4; ++g) fma8(acc, pe[g], vr[g * 3 + t]);
      float r0, r1;
      red_rows8(acc, r0, r1);
      o3[t] = (lane & 8) ? r1 : r0;
      __builtin_amdgcn_sched_barrier(0);
    }
    if (part >= 0) {
      st_gran3(rws, OP_OFF + ((hd * NPART + part) * 64 + dgr) * 16, o3[0], o3[1], o3[2], T);
      PRIO_LO();
      lds_arrive(L + L_FLAG + F_DONE, lane);
      STAMP(5);
      continue;
    }
#pragma unroll
    for (int t = 0; t < 3; ++t) Lf[L_O + (su * 3 + t) * 64 + dgr] = o3[t];
    STAMP(2);
    if (lds_arrive(L + L_FLAG + F_SARR, lane) == NS - 1) {
      const float mj = Lf[L_M + (lane < NS ? lane : 0)];
      const float M = max_rows(max8(mj));
      const float ej = __builtin_amdgcn_exp2f((mj - M) * LOG2E);
      float s0 = 0.f, s1 = 0.f, s2 = 0.f;
#pragma unroll
      for (int u = 0; u < NS; ++u) {
        const float e = __builtin_bit_cast(float, __builtin_amdgcn_readlane(__builtin_bit_cast(int, ej), u));
        s0 = fmaf(e, Lf[L_O + (u * 3 + 0) * 64 + lane], s0);
        s1 = fmaf(e, Lf[L_O + (u * 3 + 1) * 64 + lane], s1);
        s2 = fmaf(e, Lf[L_O + (u * 3 + 2) * 64 + lane], s2);
      }
      st_gran3(rws, OP_OFF + ((hd * NPART + sp) * 64 + lane) * 16, s0, s1, s2, T);
      lds_arrive(L + L_FLAG + F_DONE, lane);
      STAMP(3);
    }
    PRIO_LO();
    su = -1;
  }

  const int jw = no_units ? 16 : (int)lds_arrive(L + L_FLAG + F_SLOT, lane);
  const int rg = sp >> 1, ch = sp & 1;
  if (jw < 5) {
    wait_all_issued(L);
    WO_WAIT();
    STAMP(4);
    const int cb = 5 * ch + jw;
    const rsrc_t rwo = mk_rsrc(p.wo + (size_t)(hd * DH + 16 * rg) * D + 64 * cb, ((128 + 15) * D + 64) * 2);
    float wf[6][8];
    {
      v4u wr[6];
#pragma unroll
      for (int i = 0; i < 6; ++i) wr[i] = __builtin_amdgcn_raw_buffer_load_b128(rwo, (lane >> 3) * (D * 2) + rc_off, ((i % 3) * 64 + (i / 3) * 8) * D * 2, AUX_NT);
#pragma unroll
      for (int i = 0; i < 6; ++i)
#pragma unroll
        for (int j = 0; j < 4; ++j) { wf[i][2 * j] = bf_lo(wr[i][j]); wf[i][2 * j + 1] = bf_hi(wr[i][j]); asm volatile("" : "+v"(wf[i][2 * j]), "+v"(wf[i][2 * j + 1])); }
    }
    float y0 = 0.f, y1 = 0.f;
    if (has_ffn && cb == fk) {
      lds_wait_ge(L + L_FLAG + F_B, 1u);
      y0 = Lf[L_BB + cc2]; y1 = Lf[L_BB + cc2 + 1];
    }
    STAMP(6);
    lds_wait_ge(L + L_FLAG + F_W, NG);
    v4f oa = *(LAS v4f*)(Lf + L_GS + (lane >> 3) * 4), ob = *(LAS v4f*)(Lf + L_GS + 32 + (lane >> 3) * 4);
#pragma unroll
    for (int i = 1; i < NG; ++i) { oa += *(LAS v4f*)(Lf + L_GS + i * 64 + (lane >> 3) * 4); ob += *(LAS v4f*)(Lf + L_GS + i * 64 + 32 + (lane >> 3) * 4); }
    float acc[8];
#pragma unroll
    for (int j = 0; j < 8; ++j)
      acc[j] = fmaf(oa[2], wf[2][j], fmaf(oa[1], wf[1][j], oa[0] * wf[0][j])) + fmaf(ob[2], wf[5][j], fmaf(ob[1], wf[4][j], ob[0] * wf[3][j]));
    float r0, r1;
    red_rows8(acc, r0, r1);
    if (!(lane & 8)) st_gran2(rws, YP_OFF + ((hd * 4 + rg) * 640 + 64 * cb + cc2) * 8, y0 + r0, y1 + r1, T);
    STAMP(7);
    if (b < NRED) lds_arrive(L + L_FLAG + F_YPUB, lane);
  } else if (jw < 6 + NG) {
    wait_all_issued(L);
    lds_wait_ge(L + L_FLAG + F_DONE, 1u + __builtin_amdgcn_readfirstlane(lds_ld(L + L_FLAG + F_CLAIMED)));
    STAMP(6);
    if (jw == 5) {
      lds_wait_ge(L + L_FLAG + F_G, 1u);
      const float gate = Lf[L_G];
      const v4u g = poll1<true>(rws, ML_OFF + hd * 1024 + (lane < NPART ? lane : 0) * 16, T);
      const float mj = __uint_as_float(g[0]), lj = __uint_as_float(g[2]);
      const float M = max_rows(max8(mj));
      const float ej = __builtin_amdgcn_exp2f((mj - M) * LOG2E);
      const float Ls = sum_rows(sum8(lane < NPART ? ej * lj : 0.f));
      if (lane < NPART) Lf[L_WT + lane] = ej * (gate / Ls);
      lds_arrive(L + L_FLAG + F_WT, lane);
    } else {
      const int gi = jw - 6;
      v4u og[2];
      gather<2, false, PSLEEP>(rws, OP_OFF + ((hd * NPART + 8 * gi + (lane & 7)) * 64 + 16 * rg + (lane >> 3)) * 16, 128, T, og);
      lds_wait_ge(L + L_FLAG + F_WT, 1u);
      const float wt = Lf[L_WT + 8 * gi + (lane & 7)];
#pragma unroll
      for (int jh = 0; jh < 2; ++jh) {
        const v4f ov = {sum8(wt * __uint_as_float(og[jh][0])), sum8(wt * __uint_as_float(og[jh][1])), sum8(wt * __uint_as_float(og[jh][2])), 0.f};
        if ((lane & 7) == 0) *(LAS v4f*)(Lf + L_GS + gi * 64 + jh * 32 + (lane >> 3) * 4) = ov;
      }
      lds_arrive(L + L_FLAG + F_W, lane);
    }
    STAMP(5);
  }
  if (b < NRED && jw >= 2 && jw < 10) {
    const int v = jw - 2;
    lds_wait_ge(L + L_FLAG + F_YPUB, 1u);
    v4u gr[2];
    gather<2, true, 1>(rws, YP_OFF + ((16 * v + (lane >> 3)) * 640 + 16 * b + 2 * (lane & 7)) * 8, 8 * 640 * 8, T, gr);
    STAMP(1);
    const float a0 = sum_rows(__uint_as_float(gr[0][0]) + __uint_as_float(gr[1][0])), a1 = sum_rows(__uint_as_float(gr[0][2]) + __uint_as_float(gr[1][2]));
    if (lane < 8) { Lf[L_FIN + v * 16 + 2 * lane] = a0; Lf[L_FIN + v * 16 + 2 * lane + 1] = a1; }
    if (lds_arrive(L + L_FLAG + F_FIN, lane) == 7u) {
      if (lane < 8) {
        float s0 = 0.f, s1 = 0.f;
#pragma unroll
        for (int u = 0; u < 8; ++u) { s0 += Lf[L_FIN + u * 16 + 2 * lane]; s1 += Lf[L_FIN + u * 16 + 2 * lane + 1]; }
        ((GAS unsigned*)p.out)[8 * b + lane] = bfbits(s0) | (bfbits(s1) << 16);
      }
      if (b == 0 && lane == 0) *(GAS unsigned*)(p.ws + E_OFF) = T;
      STAMP(2);
    }
  }
  STAMP_FLUSH();
}

extern "C" void kernel_launch(void* const* d_in, const int* in_sizes, int n_in,
                              void* d_out, int out_size, void* d_ws, size_t ws_size,
                              hipStream_t stream) {
  Params p;
  p.x = (const unsigned short*)d_in[0];
  p.kc = (const unsigned short*)d_in[1];
  p.vc = (const unsigned short*)d_in[2];
  p.wq = (const unsigned short*)d_in[3];
  p.wo = (const unsigned short*)d_in[4];
  p.wg = (const unsigned short*)d_in[6];
  p.wu = (const unsigned short*)d_in[7];
  p.wd = (const unsigned short*)d_in[8];
  p.gw = (const unsigned short*)d_in[9];
  p.out = (unsigned short*)d_out;
  p.ws = (unsigned char*)d_ws;
  fused_decode<<<NWG, NTHR, 0, stream>>>(p);
}
```

```cpp
#include <hip/hip_runtime.h>
#include <hip/hip_bf16.h>
#include <stdint.h>

#define GAS __attribute__((address_space(1)))
#define LAS __attribute__((address_space(3)))
typedef unsigned v4u __attribute__((ext_vector_type(4)));
typedef float v4f __attribute__((ext_vector_type(4)));
typedef __bf16 bf16x2 __attribute__((ext_vector_type(2)));
typedef __amdgpu_buffer_rsrc_t rsrc_t;

#define LSLEEP 1
#define QDELAY 40
#define WO_EARLY 0
#define POLL_LATE 1
#define WO_SLACK 1
#define WO_DELTA 0
#define PSLEEP 1
#define MLSLEEP 2
#define LOOKAHEAD 7

constexpr int D = 640, HQ = 32, DH = 192, SK = 32768, WIN = 4096, FF = 1536;
constexpr int NWG = 256, NTHR = 1024;
constexpr int NRED = 40;
constexpr int UK = 32;
#define NSTATIC 13
constexpr int NS = NSTATIC;
constexpr int NP = WIN / UK - 8 * NS;
constexpr int NPART = 8 + NP;
static_assert(NPART % 8 == 0, "gather shape");
constexpr int E_OFF = 0;
constexpr int CNT_OFF = 4096;
constexpr int QP_OFF = CNT_OFF + 32 * 128;
constexpr int GP_OFF = QP_OFF + 32 * 3072;
constexpr int ML_OFF = GP_OFF + 24 * 2048;
constexpr int OP_OFF = ML_OFF + 32 * 1024;
constexpr int YP_OFF = OP_OFF + 32 * NPART * 1024;
constexpr int WS_BYTES = YP_OFF + 128 * 640 * 8;
constexpr int AUX_NT = 2, AUX_SC1 = 16;
constexpr unsigned SPIN_CAP = 1u << 20;
constexpr float LOG2E = 1.4426950408889634f;
constexpr int NG = NPART / 8;

constexpr int L_FLAG = 0;
constexpr int F_Q = 0, F_ISS = 1, F_ISS_S = 2, F_TICKET = 3, F_EMPTY = 4, F_SARR = 5, F_G = 6, F_B = 7, F_FIN = 8, F_YPUB = 9, F_SML = 10, F_W = 11,
              F_CLAIMED = 12, F_SLOT = 13, F_DONE = 14, F_WT = 15, F_P1 = 16, F_TE = 18;
constexpr int L_Q = 32;
constexpr int L_M = 128;
constexpr int L_L = 144;
constexpr int L_G = 160;
constexpr int L_BB = 164;
constexpr int L_FIN = 228;
constexpr int L_WT = 356;
constexpr int L_GS = 396;
constexpr int L_P1 = L_GS + 5 * 64;
constexpr int L_O = L_P1 + 512;
constexpr int L_TOTAL = L_O + NS * 192;

__device__ __forceinline__ rsrc_t mk_rsrc(const void* p, int bytes) {
  return __builtin_amdgcn_make_buffer_rsrc((void*)p, 0, bytes, 0x00020000);
}
__device__ __forceinline__ float bf_lo(unsigned u) { return __uint_as_float(u << 16); }
__device__ __forceinline__ float bf_hi(unsigned u) { return __uint_as_float(u & 0xffff0000u); }
__device__ __forceinline__ float rbf(float f) { return __bfloat162float(__float2bfloat16(f)); }
__device__ __forceinline__ unsigned bfbits(float f) { return (unsigned)__builtin_bit_cast(unsigned short, __float2bfloat16(f)); }
__device__ __forceinline__ float dot2(unsigned a, unsigned b, float c) {
  return __builtin_amdgcn_fdot2_f32_bf16(__builtin_bit_cast(bf16x2, a), __builtin_bit_cast(bf16x2, b), c, false);
}
template <int CTRL> __device__ __forceinline__ float dpp(float x) {
  return __builtin_bit_cast(float, __builtin_amdgcn_mov_dpp(__builtin_bit_cast(int, x), CTRL, 0xf, 0xf, true));
}
constexpr int XOR1 = 0xB1, XOR2 = 0x4E, XOR7 = 0x141, XOR8 = 0x128;
__device__ __forceinline__ float swap16_add(float a, float b) {
  auto s = __builtin_amdgcn_permlane16_swap(__float_as_uint(a), __float_as_uint(b), false, false);
  return __uint_as_float(s[0]) + __uint_as_float(s[1]);
}
__device__ __forceinline__ float swap32_add(float a, float b) {
  auto s = __builtin_amdgcn_permlane32_swap(__float_as_uint(a), __float_as_uint(b), false, false);
  return __uint_as_float(s[0]) + __uint_as_float(s[1]);
}
__device__ __forceinline__ float swap16_max(float a) {
  auto s = __builtin_amdgcn_permlane16_swap(__float_as_uint(a), __float_as_uint(a), false, false);
  return fmaxf(__uint_as_float(s[0]), __uint_as_float(s[1]));
}
__device__ __forceinline__ float swap32_max(float a) {
  auto s = __builtin_amdgcn_permlane32_swap(__float_as_uint(a), __float_as_uint(a), false, false);
  return fmaxf(__uint_as_float(s[0]), __uint_as_float(s[1]));
}
__device__ __forceinline__ float sum8(float x) { x += dpp<XOR1>(x); x += dpp<XOR2>(x); x += dpp<XOR7>(x); return x; }
__device__ __forceinline__ float max8(float x) { x = fmaxf(x, dpp<XOR1>(x)); x = fmaxf(x, dpp<XOR2>(x)); return fmaxf(x, dpp<XOR7>(x)); }
__device__ __forceinline__ float sum_rows(float x) { x += dpp<XOR8>(x); x = swap16_add(x, x); return swap32_add(x, x); }
__device__ __forceinline__ float max_rows(float x) { x = fmaxf(x, dpp<XOR8>(x)); x = swap16_max(x); return swap32_max(x); }
__device__ __forceinline__ void red_rows8(const float (&a)[8], float& r0, float& r1) {
  float z[4];
#pragma unroll
  for (int j = 0; j < 4; ++j) z[j] = swap16_add(a[j], a[j + 4]);
  r0 = swap32_add(z[0], z[2]); r1 = swap32_add(z[1], z[3]);
  r0 += dpp<XOR8>(r0); r1 += dpp<XOR8>(r1);
}
__device__ __forceinline__ void fma8(float (&acc)[8], float s, v4u w) {
#pragma unroll
  for (int j = 0; j < 4; ++j) { acc[2 * j] = fmaf(s, bf_lo(w[j]), acc[2 * j]); acc[2 * j + 1] = fmaf(s, bf_hi(w[j]), acc[2 * j + 1]); }
}
#define WG_BARRIER() do { __builtin_amdgcn_sched_barrier(0); asm volatile("s_barrier" ::: "memory"); __builtin_amdgcn_sched_barrier(0); } while (0)
#define PRIO_HI() __builtin_amdgcn_s_setprio(3)
#define PRIO_LO() do { __builtin_amdgcn_sched_barrier(0); __builtin_amdgcn_s_setprio(0); } while (0)
#define CBAR() asm volatile("" ::: "memory")
#define LGKM0() asm volatile("s_waitcnt lgkmcnt(0)" ::: "memory")
__device__ __forceinline__ unsigned lds_ld(LAS unsigned* p) { return __hip_atomic_load(p, __ATOMIC_RELAXED, __HIP_MEMORY_SCOPE_WORKGROUP); }
__device__ __forceinline__ void lds_wait_ge(LAS unsigned* p, unsigned want) {
#pragma unroll 1
  for (unsigned s = 0; s < SPIN_CAP; ++s) {
    CBAR();
    if ((unsigned)__builtin_amdgcn_readfirstlane(lds_ld(p)) >= want) break;
    __builtin_amdgcn_s_sleep(LSLEEP);
  }
  CBAR();
}
__device__ __forceinline__ unsigned lds_arrive(LAS unsigned* p, int lane) {
  LGKM0();
  unsigned old = 0;
  if (lane == 0) old = __hip_atomic_fetch_add(p, 1u, __ATOMIC_RELAXED, __HIP_MEMORY_SCOPE_WORKGROUP);
  old = __builtin_amdgcn_readfirstlane(old);
  CBAR();
  return old;
}
__device__ __forceinline__ void wait_all_issued(LAS unsigned* L) {
#pragma unroll 1
  for (unsigned s = 0; s < SPIN_CAP; ++s) {
    CBAR();
    const unsigned e = __builtin_amdgcn_readfirstlane(lds_ld(L + L_FLAG + F_EMPTY));
    const unsigned c = __builtin_amdgcn_readfirstlane(lds_ld(L + L_FLAG + F_CLAIMED));
    const unsigned n = __builtin_amdgcn_readfirstlane(lds_ld(L + L_FLAG + F_ISS));
    if (e && n + WO_EARLY >= 4 * (NS + c)) break;
    __builtin_amdgcn_s_sleep(LSLEEP);
  }
  CBAR();
}
__device__ __forceinline__ void wait_done(LAS unsigned* L, unsigned slack) {
#pragma unroll 1
  for (unsigned s = 0; s < SPIN_CAP; ++s) {
    CBAR();
    const unsigned e = __builtin_amdgcn_readfirstlane(lds_ld(L + L_FLAG + F_EMPTY));
    const unsigned c = __builtin_amdgcn_readfirstlane(lds_ld(L + L_FLAG + F_CLAIMED));
    const unsigned n = __builtin_amdgcn_readfirstlane(lds_ld(L + L_FLAG + F_DONE));
    if (e && n + slack >= 1u + c) break;
    __builtin_amdgcn_s_sleep(LSLEEP);
  }
  CBAR();
}
__device__ __forceinline__ void st_gran2(rsrc_t rs, int off, float a, float b, unsigned T) {
  v4u g = {__float_as_uint(a), T, __float_as_uint(b), T};
  __builtin_amdgcn_raw_buffer_store_b128(g, rs, off, 0, AUX_SC1);
}
__device__ __forceinline__ void st_gran3(rsrc_t rs, int off, float a, float b, float c, unsigned T) {
  v4u g = {__float_as_uint(a), __float_as_uint(b), __float_as_uint(c), T};
  __builtin_amdgcn_raw_buffer_store_b128(g, rs, off, 0, AUX_SC1);
}
template <int N, bool PAIR, int SLEEP>
__device__ __forceinline__ void gather(rsrc_t rs, int off, int stride, unsigned T, v4u (&gr)[N]) {
  CBAR();
#pragma unroll
  for (int i = 0; i < N; ++i) gr[i] = __builtin_amdgcn_raw_buffer_load_b128(rs, off + i * stride, 0, AUX_SC1);
#pragma unroll 1
  for (unsigned s2 = 0; s2 < SPIN_CAP; ++s2) {
    unsigned bad = 0u;
#pragma unroll
    for (int i = 0; i < N; ++i) bad |= PAIR ? ((gr[i][1] ^ T) | (gr[i][3] ^ T)) : (gr[i][3] ^ T);
    if (__all(bad == 0u)) break;
    __builtin_amdgcn_s_sleep(SLEEP);
    CBAR();
#pragma unroll
    for (int i = 0; i < N; ++i) {
      const bool have = PAIR ? (((gr[i][1] ^ T) | (gr[i][3] ^ T)) == 0u) : (gr[i][3] == T);
      if (!have) gr[i] = __builtin_amdgcn_raw_buffer_load_b128(rs, off + i * stride, 0, AUX_SC1);
    }
  }
  CBAR();
}
template <bool PAIR>
__device__ __forceinline__ v4u poll1(rsrc_t rs, int off, unsigned T) {
  CBAR();
  v4u g = __builtin_amdgcn_raw_buffer_load_b128(rs, off, 0, AUX_SC1);
#pragma unroll 1
  for (unsigned s2 = 0; s2 < SPIN_CAP; ++s2) {
    const bool miss = PAIR ? (((g[1] ^ T) | (g[3] ^ T)) != 0u) : (g[3] != T);
    if (!__any(miss)) break;
    __builtin_amdgcn_s_sleep(PSLEEP);
    CBAR();
    if (miss) g = __builtin_amdgcn_raw_buffer_load_b128(rs, off, 0, AUX_SC1);
  }
  CBAR();
  return g;
}
template <int NI>
__device__ __forceinline__ void gemv_rows(const v4u (&wr)[NI], unsigned xv, int lane, float& r0, float& r1) {
  float acc[8] = {0.f, 0.f, 0.f, 0.f, 0.f, 0.f, 0.f, 0.f};
  const int r = lane >> 3;
#pragma unroll
  for (int i = 0; i < NI; ++i) {
    const unsigned hv = (unsigned)__builtin_amdgcn_ds_bpermute((4 * i + (r >> 1)) * 4, (int)xv);
    fma8(acc, (r & 1) ? bf_hi(hv) : bf_lo(hv), wr[i]);
  }
  red_rows8(acc, r0, r1);
}
#define STAMP(k) do {} while (0)
#define STAMP_FLUSH() do {} while (0)
struct Params {
  const unsigned short *x, *kc, *vc, *wq, *wo, *wg, *wu, *wd, *gw;
  unsigned short* out;
  unsigned char* ws;
};

__global__ void __launch_bounds__(NTHR, 4) fused_decode(Params p) {
  __shared__ __attribute__((aligned(16))) unsigned lds_raw[L_TOTAL];
  LAS unsigned* L = (LAS unsigned*)lds_raw;
  LAS float* Lf = (LAS float*)lds_raw;
  const int b = blockIdx.x, tid = threadIdx.x, lane = tid & 63;
  const int w = __builtin_amdgcn_readfirstlane(tid >> 6);
  STAMP(0);
  if (tid < 32) L[L_FLAG + tid] = 0u;
#define WO_WAIT() wait_done(L, WO_SLACK)
  asm volatile("s_waitcnt lgkmcnt(0)" :: "s"(p.x), "s"(p.kc), "s"(p.vc), "s"(p.wq), "s"(p.wo), "s"(p.wg), "s"(p.wu), "s"(p.wd), "s"(p.gw), "s"(p.out), "s"(p.ws) : "memory");
  const int hd = b >> 3, sp = b & 7;
  const rsrc_t rws = mk_rsrc(p.ws, WS_BYTES);

  unsigned code_touch = 0u;
  if (w == 0) {
    unsigned long long pc; unsigned len;
    asm volatile("s_getpc_b64 %0\n\t.Lmk_code_a:\n\ts_mov_b32 %1, .Lmk_code_z-.Lmk_code_a" : "=s"(pc), "=s"(len));
    const rsrc_t rc = mk_rsrc((const void*)pc, (int)len);
    code_touch = __builtin_amdgcn_raw_buffer_load_b32(rc, ((b >> 3) & 7) * 4096 + lane * 64, 0, 0);
  }
  const unsigned Eraw = *(const __attribute__((address_space(4))) unsigned*)(p.ws + E_OFF);
#define GET_TAG() ({ unsigned t_; asm volatile("s_add_u32 %0, %1, 1" : "=s"(t_) : "s"(Eraw) : "scc"); t_; })
  const int cc2 = 8 * (lane & 7) + 4 * ((lane >> 4) & 1) + 2 * (lane >> 5);
  const int rc_off = (lane & 7) * 16;
  unsigned T = 0u;

  if (w >= 4) WG_BARRIER();
  if (w < 4) {
    const int nround = (sp == 6 && w >= 2) ? 3 : 1;
#pragma unroll 1
    for (int round = 0; round < nround; ++round) {
      const bool p1a = round == 0;
      const int wv = p1a ? w : 2 * (w - 2) + round - 1;
      const bool isq = p1a && sp < 6;
      const int f = p1a ? hd * 2 + (sp - 6) : 64 + hd;
      const int half = isq ? (sp & 1) : (f & 1);
      const int row0 = 320 * half + 80 * wv;
      const int ld = isq ? HQ * DH : FF;
      const unsigned short* base = isq ? p.wq + hd * DH + 64 * (sp >> 1) : ((f & 2) ? p.wu : p.wg) + 64 * (f >> 2);
      const int pub = isq ? QP_OFF + (hd * 6 + sp) * 512 : GP_OFF + f * 512;
      if (w == 0 && sp == 0 && lane == 0) __builtin_amdgcn_raw_buffer_store_b32(0u, rws, CNT_OFF + hd * 128, 0, AUX_SC1);
      const rsrc_t rq = mk_rsrc(base + (size_t)row0 * ld, (79 * ld + 64) * 2);
      const unsigned xq = ((const GAS unsigned*)p.x)[(row0 >> 1) + (lane < 40 ? lane : 39)];
      v4u wr[10];
#pragma unroll
      for (int i = 0; i < 10; ++i) wr[i] = __builtin_amdgcn_raw_buffer_load_b128(rq, (lane >> 3) * (ld * 2) + rc_off, i * 16 * ld, AUX_NT);
      if (p1a) WG_BARRIER();
      float r0, r1;
      gemv_rows<10>(wr, xq, lane, r0, r1);
      asm volatile("" :: "v"(code_touch));
      if (p1a) T = GET_TAG();
      asm volatile("s_waitcnt vmcnt(0)" ::: "memory");
      LAS float* slot = Lf + L_P1 + (p1a ? 0 : 256);
      if (!(lane & 8)) { slot[wv * 64 + cc2] = r0; slot[wv * 64 + cc2 + 1] = r1; }
      if (lds_arrive(L + L_FLAG + F_P1 + (p1a ? 0 : 1), lane) == 3u && lane < 32) {
        const float s0 = (slot[2 * lane] + slot[64 + 2 * lane]) + (slot[128 + 2 * lane] + slot[192 + 2 * lane]);
        const float s1 = (slot[2 * lane + 1] + slot[65 + 2 * lane]) + (slot[129 + 2 * lane] + slot[193 + 2 * lane]);
        st_gran2(rws, pub + lane * 16, s0, s1, T);
      }
    }
  }
  const int fc = (b >> 1) / 5, fk = 5 * (b & 1) + (b >> 1) % 5;
  const bool has_ffn = b < 240;
  if (w == 0) {
    STAMP(1);
    __builtin_amdgcn_s_sleep(QDELAY);
    {
      v4u gr[3];
      gather<3, true, 8>(rws, QP_OFF + hd * 3072 + lane * 16, 1024, T, gr);
#pragma unroll
      for (int c = 0; c < 3; ++c) {
        const float q0 = swap32_add(__uint_as_float(gr[c][0]), __uint_as_float(gr[c][0])), q1 = swap32_add(__uint_as_float(gr[c][2]), __uint_as_float(gr[c][2]));
        if (lane < 32) L[L_Q + 32 * c + lane] = bfbits(q0) | (bfbits(q1) << 16);
      }
    }
    lds_arrive(L + L_FLAG + F_Q, lane);
    STAMP(2);
    float z = 0.f;
#pragma unroll
    for (int i = 0; i < 5; ++i) {
      const unsigned xv = ((const GAS unsigned*)p.x)[lane + 64 * i], gv = ((const GAS unsigned*)p.gw)[lane + 64 * i];
      z += rbf(bf_lo(xv) * bf_lo(gv)); z += rbf(bf_hi(xv) * bf_hi(gv));
    }
    z = rbf(sum_rows(sum8(z)));
    const float gate = rbf(1.f / (1.f + __expf(-z)));
    if (lane == 0) { Lf[L_G] = gate; Lf[L_G + 1] = rbf(1.f - gate); }
    lds_arrive(L + L_FLAG + F_G, lane);
    STAMP(3);
  } else if (w == 3 && has_ffn) {
    lds_wait_ge(L + L_FLAG + F_ISS, 4 * NS - 4);
    unsigned hp;
    {
      v4u gr[2];
      gather<2, true, 16>(rws, GP_OFF + fc * 2048 + lane * 16, 1024, T, gr);
      const float g0 = rbf(swap32_add(__uint_as_float(gr[0][0]), __uint_as_float(gr[0][0]))), g1 = rbf(swap32_add(__uint_as_float(gr[0][2]), __uint_as_float(gr[0][2])));
      const float u0 = rbf(swap32_add(__uint_as_float(gr[1][0]), __uint_as_float(gr[1][0]))), u1 = rbf(swap32_add(__uint_as_float(gr[1][2]), __uint_as_float(gr[1][2])));
      hp = bfbits((g0 > 0.f ? g0 : 0.f) * u0) | (bfbits((g1 > 0.f ? g1 : 0.f) * u1) << 16);
    }
    STAMP(2);
    wait_all_issued(L);
    WO_WAIT();
    const rsrc_t rwd = mk_rsrc(p.wd + (size_t)(64 * fc) * D + 64 * fk, (63 * D + 64) * 2);
    v4u wr[8];
#pragma unroll
    for (int i = 0; i < 8; ++i) wr[i] = __builtin_amdgcn_raw_buffer_load_b128(rwd, (lane >> 3) * (D * 2) + rc_off, i * 8 * D * 2, AUX_NT);
    __builtin_amdgcn_sched_barrier(0);
    float r2, r3;
    gemv_rows<8>(wr, hp, lane, r2, r3);
    lds_wait_ge(L + L_FLAG + F_G, 1u);
    const float omg = Lf[L_G + 1];
    if (!(lane & 8)) { Lf[L_BB + cc2] = omg * r2; Lf[L_BB + cc2 + 1] = omg * r3; }
    lds_arrive(L + L_FLAG + F_B, lane);
    STAMP(3);
  }

  int su = w >= 4 ? w - 4 : ((w == 1 || w == 2) && 11 + w < NS ? 11 + w : -1);
  const int koff = (lane >> 3) * 384 + rc_off;
  const int dgr = cc2 + ((lane >> 3) & 1);
  const bool no_units = w == 3;
#pragma unroll 1
  for (; !no_units;) {
    int key0, part;
    if (su >= 0) {
      key0 = sp * (NS * UK) + su * UK; part = -1;
    } else {
      const unsigned tk = lds_arrive(L + L_FLAG + F_TICKET, lane);
      bool empty = false;
#pragma unroll 1
      for (unsigned s = 0; s < SPIN_CAP; ++s) {
        CBAR();
        const unsigned e = __builtin_amdgcn_readfirstlane(lds_ld(L + L_FLAG + F_EMPTY));
        const unsigned n = __builtin_amdgcn_readfirstlane(lds_ld(L + L_FLAG + F_ISS));
        const unsigned q = __builtin_amdgcn_readfirstlane(lds_ld(L + L_FLAG + F_Q));
        if (e) { empty = true; break; }
        if (q && n + LOOKAHEAD >= 4 * (NS + tk)) break;
        __builtin_amdgcn_s_sleep(LSLEEP);
      }
      if (empty) break;
      unsigned u = 0;
      if (lane == 0) u = __hip_atomic_fetch_add((GAS unsigned*)(p.ws + CNT_OFF + hd * 128), 1u, __ATOMIC_RELAXED, __HIP_MEMORY_SCOPE_AGENT);
      u = __builtin_amdgcn_readfirstlane(u);
      if (u >= (unsigned)NP) {
        lds_arrive(L + L_FLAG + F_EMPTY, lane); break; }
      key0 = 8 * NS * UK + (int)u * UK; part = 8 + (int)u;
      lds_arrive(L + L_FLAG + F_CLAIMED, lane);
      lds_wait_ge(L + L_FLAG + F_ISS_S, NS);
      STAMP(4);
    }
    const size_t kv_off = ((size_t)hd * SK + (SK - WIN) + key0) * DH;
    const rsrc_t rk = mk_rsrc(p.kc + kv_off, UK * DH * 2);
    const rsrc_t rv = mk_rsrc(p.vc + kv_off, UK * DH * 2);
    v4u kr[12], vr[12];
#define TICK() do { if (lane == 0) __hip_atomic_fetch_add(L + L_FLAG + F_ISS, 1u, __ATOMIC_RELAXED, __HIP_MEMORY_SCOPE_WORKGROUP); __builtin_amdgcn_sched_barrier(0); } while (0)
#pragma unroll
    for (int g = 0; g < 4; ++g) {
#pragma unroll
      for (int t = 0; t < 3; ++t) { kr[g * 3 + t] = __builtin_amdgcn_raw_buffer_load_b128(rk, koff + t * 128, g * 3072, AUX_NT); __builtin_amdgcn_sched_barrier(0); }
      if (g & 1) TICK();
    }
#pragma unroll
    for (int i = 0; i < 12; ++i) {
      const int t = i >> 2, g = i & 3;
      vr[g * 3 + t] = __builtin_amdgcn_raw_buffer_load_b128(rv, koff + t * 128, g * 3072, AUX_NT); __builtin_amdgcn_sched_barrier(0);
      if (i == 5 || i == 11) TICK();
    }
    if (part < 0) { if (w >= 4) T = GET_TAG(); lds_arrive(L + L_FLAG + F_ISS_S, lane); STAMP(1); }
    lds_wait_ge(L + L_FLAG + F_Q, 1u);
    float pe[4], mu, lu;
    {
      v4u qf[3];
#pragma unroll
      for (int t = 0; t < 3; ++t) qf[t] = *(LAS v4u*)(L + L_Q + 32 * t + 4 * (lane & 7));
      float s[4];
#pragma unroll
      for (int g = 0; g < 4; ++g) {
        float a = 0.f;
#pragma unroll
        for (int t = 0; t < 3; ++t)
#pragma unroll
          for (int j = 0; j < 4; ++j) a = dot2(kr[g * 3 + t][j], qf[t][j], a);
        s[g] = rbf(rbf(sum8(a)) * 0.07216878364870322f);
      }
      mu = max_rows(fmaxf(fmaxf(s[0], s[1]), fmaxf(s[2], s[3])));
#pragma unroll
      for (int g = 0; g < 4; ++g) pe[g] = __builtin_amdgcn_exp2f((s[g] - mu) * LOG2E);
      lu = sum_rows((pe[0] + pe[1]) + (pe[2] + pe[3]));
    }
    PRIO_HI();
    {
      float pm = mu, pl = lu;
      int slot = part;
      if (part < 0) {
        if (lane == 0) { Lf[L_M + su] = mu; Lf[L_L + su] = lu; }
        if (lds_arrive(L + L_FLAG + F_SML, lane) == NS - 1) {
          const float mj = Lf[L_M + (lane < NS ? lane : 0)], lj = Lf[L_L + (lane < NS ? lane : 0)];
          pm = max_rows(max8(mj));
          pl = sum_rows(sum8(lane < NS ? __builtin_amdgcn_exp2f((mj - pm) * LOG2E) * lj : 0.f));
          slot = sp;
        }
      }
      st_gran2(rws, (slot >= 0 && lane == 0) ? ML_OFF + hd * 1024 + slot * 16 : 0x7ffffff0, pm, pl, T);
      __builtin_amdgcn_sched_barrier(0);
    }
    float o3[3];
#pragma unroll
    for (int t = 0; t < 3; ++t) {
      float acc[8] = {0.f, 0.f, 0.f, 0.f, 0.f, 0.f, 0.f, 0.f};
#pragma unroll
      for (int g = 0; g < 4; ++g) fma8(acc, pe[g], vr[g * 3 + t]);
      float r0, r1;
      red_rows8(acc, r0, r1);
      o3[t] = (lane & 8) ? r1 : r0;
      __builtin_amdgcn_sched_barrier(0);
    }
    if (part >= 0) {
      st_gran3(rws, OP_OFF + ((hd * NPART + part) * 64 + dgr) * 16, o3[0], o3[1], o3[2], T);
      PRIO_LO();
      lds_arrive(L + L_FLAG + F_DONE, lane);
      STAMP(5);
      continue;
    }
#pragma unroll
    for (int t = 0; t < 3; ++t) Lf[L_O + (su * 3 + t) * 64 + dgr] = o3[t];
    STAMP(2);
    if (lds_arrive(L + L_FLAG + F_SARR, lane) == NS - 1) {
      const float mj = Lf[L_M + (lane < NS ? lane : 0)];
      const float M = max_rows(max8(mj));
      const float ej = __builtin_amdgcn_exp2f((mj - M) * LOG2E);
      float s0 = 0.f, s1 = 0.f, s2 = 0.f;
#pragma unroll
      for (int u = 0; u < NS; ++u) {
        const float e = __builtin_bit_cast(float, __builtin_amdgcn_readlane(__builtin_bit_cast(int, ej), u));
        s0 = fmaf(e, Lf[L_O + (u * 3 + 0) * 64 + lane], s0);
        s1 = fmaf(e, Lf[L_O + (u * 3 + 1) * 64 + lane], s1);
        s2 = fmaf(e, Lf[L_O + (u * 3 + 2) * 64 + lane], s2);
      }
      st_gran3(rws, OP_OFF + ((hd * NPART + sp) * 64 + lane) * 16, s0, s1, s2, T);
      lds_arrive(L + L_FLAG + F_DONE, lane);
      STAMP(3);
    }
    PRIO_LO();
    su = -1;
  }

  const int jw = no_units ? 16 : (int)lds_arrive(L + L_FLAG + F_SLOT, lane);
  const int rg = sp >> 1, ch = sp & 1;
  if (jw < 5) {
    wait_all_issued(L);
    WO_WAIT();
    STAMP(4);
    const int cb = 5 * ch + jw;
    const rsrc_t rwo = mk_rsrc(p.wo + (size_t)(hd * DH + 16 * rg) * D + 64 * cb, ((128 + 15) * D + 64) * 2);
    float wf[6][8];
    {
      v4u wr[6];
#pragma unroll
      for (int i = 0; i < 6; ++i) wr[i] = __builtin_amdgcn_raw_buffer_load_b128(rwo, (lane >> 3) * (D * 2) + rc_off, ((i % 3) * 64 + (i / 3) * 8) * D * 2, AUX_NT);
#pragma unroll
      for (int i = 0; i < 6; ++i)
#pragma unroll
        for (int j = 0; j < 4; ++j) { wf[i][2 * j] = bf_lo(wr[i][j]); wf[i][2 * j + 1] = bf_hi(wr[i][j]); asm volatile("" : "+v"(wf[i][2 * j]), "+v"(wf[i][2 * j + 1])); }
    }
    float y0 = 0.f, y1 = 0.f;
    if (has_ffn && cb == fk) {
      lds_wait_ge(L + L_FLAG + F_B, 1u);
      y0 = Lf[L_BB + cc2]; y1 = Lf[L_BB + cc2 + 1];
    }
    STAMP(6);
    lds_wait_ge(L + L_FLAG + F_W, NG);
    v4f oa = *(LAS v4f*)(Lf + L_GS + (lane >> 3) * 4), ob = *(LAS v4f*)(Lf + L_GS + 32 + (lane >> 3) * 4);
#pragma unroll
    for (int i = 1; i < NG; ++i) { oa += *(LAS v4f*)(Lf + L_GS + i * 64 + (lane >> 3) * 4); ob += *(LAS v4f*)(Lf + L_GS + i * 64 + 32 + (lane >> 3) * 4); }
    float acc[8];
#pragma unroll
    for (int j = 0; j < 8; ++j)
      acc[j] = fmaf(oa[2], wf[2][j], fmaf(oa[1], wf[1][j], oa[0] * wf[0][j])) + fmaf(ob[2], wf[5][j], fmaf(ob[1], wf[4][j], ob[0] * wf[3][j]));
    float r0, r1;
    red_rows8(acc, r0, r1);
    if (!(lane & 8)) st_gran2(rws, YP_OFF + ((hd * 4 + rg) * 640 + 64 * cb + cc2) * 8, y0 + r0, y1 + r1, T);
    STAMP(7);
    if (b < NRED) lds_arrive(L + L_FLAG + F_YPUB, lane);
  } else if (jw < 6 + NG) {
    wait_all_issued(L);
    lds_wait_ge(L + L_FLAG + F_DONE, 1u + __builtin_amdgcn_readfirstlane(lds_ld(L + L_FLAG + F_CLAIMED)));
    STAMP(6);
    if (jw == 5) {
      lds_wait_ge(L + L_FLAG + F_G, 1u);
      const float gate = Lf[L_G];
      const v4u g = poll1<true>(rws, ML_OFF + hd * 1024 + (lane < NPART ? lane : 0) * 16, T);
      const float mj = __uint_as_float(g[0]), lj = __uint_as_float(g[2]);
      const float M = max_rows(max8(mj));
      const float ej = __builtin_amdgcn_exp2f((mj - M) * LOG2E);
      const float Ls = sum_rows(sum8(lane < NPART ? ej * lj : 0.f));
      if (lane < NPART) Lf[L_WT + lane] = ej * (gate / Ls);
      lds_arrive(L + L_FLAG + F_WT, lane);
    } else {
      const int gi = jw - 6;
      v4u og[2];
      gather<2, false, PSLEEP>(rws, OP_OFF + ((hd * NPART + 8 * gi + (lane & 7)) * 64 + 16 * rg + (lane >> 3)) * 16, 128, T, og);
      lds_wait_ge(L + L_FLAG + F_WT, 1u);
      const float wt = Lf[L_WT + 8 * gi + (lane & 7)];
#pragma unroll
      for (int jh = 0; jh < 2; ++jh) {
        const v4f ov = {sum8(wt * __uint_as_float(og[jh][0])), sum8(wt * __uint_as_float(og[jh][1])), sum8(wt * __uint_as_float(og[jh][2])), 0.f};
        if ((lane & 7) == 0) *(LAS v4f*)(Lf + L_GS + gi * 64 + jh * 32 + (lane >> 3) * 4) = ov;
      }
      lds_arrive(L + L_FLAG + F_W, lane);
    }
    STAMP(5);
  }
  if (b < NRED && jw >= 2 && jw < 10) {
    const int v = jw - 2;
    lds_wait_ge(L + L_FLAG + F_YPUB, 1u);
    v4u gr[2];
    gather<2, true, 1>(rws, YP_OFF + ((16 * v + (lane >> 3)) * 640 + 16 * b + 2 * (lane & 7)) * 8, 8 * 640 * 8, T, gr);
    STAMP(1);
    const float a0 = sum_rows(__uint_as_float(gr[0][0]) + __uint_as_float(gr[1][0])), a1 = sum_rows(__uint_as_float(gr[0][2]) + __uint_as_float(gr[1][2]));
    if (lane < 8) { Lf[L_FIN + v * 16 + 2 * lane] = a0; Lf[L_FIN + v * 16 + 2 * lane + 1] = a1; }
    if (lds_arrive(L + L_FLAG + F_FIN, lane) == 7u) {
      if (lane < 8) {
        float s0 = 0.f, s1 = 0.f;
#pragma unroll
        for (int u = 0; u < 8; ++u) { s0 += Lf[L_FIN + u * 16 + 2 * lane]; s1 += Lf[L_FIN + u * 16 + 2 * lane + 1]; }
        ((GAS unsigned*)p.out)[8 * b + lane] = bfbits(s0) | (bfbits(s1) << 16);
      }
      if (b == 0 && lane == 0) *(GAS unsigned*)(p.ws + E_OFF) = T;
      STAMP(2);
    }
  }
  STAMP_FLUSH();
  asm volatile(".Lmk_code_z:");
}

extern "C" void kernel_launch(void* const* d_in, const int* in_sizes, int n_in,
                              void* d_out, int out_size, void* d_ws, size_t ws_size,
                              hipStream_t stream) {
  Params p;
  p.x = (const unsigned short*)d_in[0];
  p.kc = (const unsigned short*)d_in[1];
  p.vc = (const unsigned short*)d_in[2];
  p.wq = (const unsigned short*)d_in[3];
  p.wo = (const unsigned short*)d_in[4];
  p.wg = (const unsigned short*)d_in[6];
  p.wu = (const unsigned short*)d_in[7];
  p.wd = (const unsigned short*)d_in[8];
  p.gw = (const unsigned short*)d_in[9];
  p.out = (unsigned short*)d_out;
  p.ws = (unsigned char*)d_ws;
  fused_decode<<<NWG, NTHR, 0, stream>>>(p);
}
```

```cpp
#include <hip/hip_runtime.h>
#include <hip/hip_bf16.h>
#include <stdint.h>

#define GAS __attribute__((address_space(1)))
#define LAS __attribute__((address_space(3)))
typedef unsigned v4u __attribute__((ext_vector_type(4)));
typedef float v4f __attribute__((ext_vector_type(4)));
typedef float v2f __attribute__((ext_vector_type(2)));
typedef __bf16 bf16x2 __attribute__((ext_vector_type(2)));
typedef __amdgpu_buffer_rsrc_t rsrc_t;

#define LSLEEP 1
#define QDELAY 127
#define WO_EARLY 0
#define POLL_LATE 1
#define WO_SLACK 0
#define WO_DELTA 0
#define EARLY_BAR 0
#define QDELAY2 0
#define P1PRIO 0
#define WO_CAP 18
#define EARLY0 1
#define ESLEEP 10
#define EARLYG 2
#define WD_MODE 1
#define TIGHT_AT 1u
#define PSLEEP 1
#define MLSLEEP 2
#define LOOKAHEAD 7

constexpr int D = 640, HQ = 32, DH = 192, SK = 32768, WIN = 4096, FF = 1536;
constexpr int NWG = 256, NTHR = 1024;
constexpr int NRED = 40;
constexpr int UK = 32;
#define NSTATIC 13
constexpr int NS = NSTATIC;
#define NHALF 0
constexpr int NPH = NHALF;
constexpr int NPF = WIN / UK - 8 * NS - NPH / 2;
constexpr int NP = NPF + NPH;
constexpr int NPART = 8 + NP;
static_assert(NPART % 8 == 0, "gather shape");
constexpr int E_OFF = 0;
constexpr int CNT_OFF = 4096;
constexpr int QP_OFF = CNT_OFF + 32 * 128;
constexpr int GP_OFF = QP_OFF + 32 * 6144;
constexpr int ML_OFF = GP_OFF + 24 * 4096;
constexpr int OP_OFF = ML_OFF + 32 * 1024;
constexpr int YP_OFF = OP_OFF + 32 * NPART * 1024;
constexpr int WS_BYTES = YP_OFF + 128 * 640 * 8;
constexpr int AUX_NT = 2, AUX_SC1 = 16;
constexpr unsigned SPIN_CAP = 1u << 20;
constexpr float LOG2E = 1.4426950408889634f;
constexpr int NG = NPART / 8;

constexpr int L_FLAG = 0;
constexpr int F_Q = 0, F_ISS = 1, F_ISS_S = 2, F_TICKET = 3, F_EMPTY = 4, F_SARR = 5, F_G = 6, F_B = 7, F_FIN = 8, F_YPUB = 9, F_SML = 10, F_W = 11,
              F_CLAIMED = 12, F_SLOT = 13, F_DONE = 14, F_WT = 15, F_P1 = 16, F_TE = 18;
constexpr int L_Q = 32;
constexpr int L_M = 128;
constexpr int L_L = 144;
constexpr int L_G = 160;
constexpr int L_BB = 164;
constexpr int L_FIN = 228;
constexpr int L_WT = 356;
constexpr int L_GS = 396;
constexpr int L_P1 = L_GS + 8 * 64;
constexpr int L_O = L_P1 + 512;
constexpr int L_TOTAL = L_O + NS * 192;

__device__ __forceinline__ rsrc_t mk_rsrc(const void* p, int bytes) {
  return __builtin_amdgcn_make_buffer_rsrc((void*)p, 0, bytes, 0x00020000);
}
__device__ __forceinline__ float bf_lo(unsigned u) { return __uint_as_float(u << 16); }
__device__ __forceinline__ float bf_hi(unsigned u) { return __uint_as_float(u & 0xffff0000u); }
__device__ __forceinline__ float rbf(float f) { return __bfloat162float(__float2bfloat16(f)); }
__device__ __forceinline__ unsigned bfbits(float f) { return (unsigned)__builtin_bit_cast(unsigned short, __float2bfloat16(f)); }
__device__ __forceinline__ float dot2(unsigned a, unsigned b, float c) {
  return __builtin_amdgcn_fdot2_f32_bf16(__builtin_bit_cast(bf16x2, a), __builtin_bit_cast(bf16x2, b), c, false);
}
template <int CTRL> __device__ __forceinline__ float dpp(float x) {
  return __builtin_bit_cast(float, __builtin_amdgcn_mov_dpp(__builtin_bit_cast(int, x), CTRL, 0xf, 0xf, true));
}
constexpr int XOR1 = 0xB1, XOR2 = 0x4E, XOR7 = 0x141, XOR8 = 0x128;
__device__ __forceinline__ float swap16_add(float a, float b) {
  auto s = __builtin_amdgcn_permlane16_swap(__float_as_uint(a), __float_as_uint(b), false, false);
  return __uint_as_float(s[0]) + __uint_as_float(s[1]);
}
__device__ __forceinline__ float swap32_add(float a, float b) {
  auto s = __builtin_amdgcn_permlane32_swap(__float_as_uint(a), __float_as_uint(b), false, false);
  return __uint_as_float(s[0]) + __uint_as_float(s[1]);
}
__device__ __forceinline__ float swap16_max(float a) {
  auto s = __builtin_amdgcn_permlane16_swap(__float_as_uint(a), __float_as_uint(a), false, false);
  return fmaxf(__uint_as_float(s[0]), __uint_as_float(s[1]));
}
__device__ __forceinline__ float swap32_max(float a) {
  auto s = __builtin_amdgcn_permlane32_swap(__float_as_uint(a), __float_as_uint(a), false, false);
  return fmaxf(__uint_as_float(s[0]), __uint_as_float(s[1]));
}
__device__ __forceinline__ float sum8(float x) { x += dpp<XOR1>(x); x += dpp<XOR2>(x); x += dpp<XOR7>(x); return x; }
__device__ __forceinline__ float max8(float x) { x = fmaxf(x, dpp<XOR1>(x)); x = fmaxf(x, dpp<XOR2>(x)); return fmaxf(x, dpp<XOR7>(x)); }
__device__ __forceinline__ float sum_rows(float x) { x += dpp<XOR8>(x); x = swap16_add(x, x); return swap32_add(x, x); }
__device__ __forceinline__ float max_rows(float x) { x = fmaxf(x, dpp<XOR8>(x)); x = swap16_max(x); return swap32_max(x); }
__device__ __forceinline__ void red_rows8(const float (&a)[8], float& r0, float& r1) {
  float z[4];
#pragma unroll
  for (int j = 0; j < 4; ++j) z[j] = swap16_add(a[j], a[j + 4]);
  r0 = swap32_add(z[0], z[2]); r1 = swap32_add(z[1], z[3]);
  r0 += dpp<XOR8>(r0); r1 += dpp<XOR8>(r1);
}
__device__ __forceinline__ void fma8(float (&acc)[8], float s, v4u w) {
#pragma unroll
  for (int j = 0; j < 4; ++j) { acc[2 * j] = fmaf(s, bf_lo(w[j]), acc[2 * j]); acc[2 * j + 1] = fmaf(s, bf_hi(w[j]), acc[2 * j + 1]); }
}
__device__ __forceinline__ void fma8b(float (&acc)[8], unsigned slo, unsigned shi, v4u w) {
#pragma unroll
  for (int j = 0; j < 4; ++j) { acc[2 * j] = dot2(w[j], slo, acc[2 * j]); acc[2 * j + 1] = dot2(w[j], shi, acc[2 * j + 1]); }
}
#define WG_BARRIER() do { __builtin_amdgcn_sched_barrier(0); asm volatile("s_barrier" ::: "memory"); __builtin_amdgcn_sched_barrier(0); } while (0)
#define PRIO_HI() __builtin_amdgcn_s_setprio(3)
#define PRIO_LO() do { __builtin_amdgcn_sched_barrier(0); __builtin_amdgcn_s_setprio(0); } while (0)
#define CBAR() asm volatile("" ::: "memory")
#define LGKM0() asm volatile("s_waitcnt lgkmcnt(0)" ::: "memory")
__device__ __forceinline__ unsigned lds_ld(LAS unsigned* p) { return __hip_atomic_load(p, __ATOMIC_RELAXED, __HIP_MEMORY_SCOPE_WORKGROUP); }
__device__ __forceinline__ void lds_wait_ge(LAS unsigned* p, unsigned want) {
#pragma unroll 1
  for (unsigned s = 0; s < SPIN_CAP; ++s) {
    CBAR();
    if ((unsigned)__builtin_amdgcn_readfirstlane(lds_ld(p)) >= want) break;
    __builtin_amdgcn_s_sleep(LSLEEP);
  }
  CBAR();
}
__device__ __forceinline__ unsigned lds_arrive(LAS unsigned* p, int lane) {
  LGKM0();
  unsigned old = 0;
  if (lane == 0) old = __hip_atomic_fetch_add(p, 1u, __ATOMIC_RELAXED, __HIP_MEMORY_SCOPE_WORKGROUP);
  old = __builtin_amdgcn_readfirstlane(old);
  CBAR();
  return old;
}
__device__ __forceinline__ void lds_signal(LAS unsigned* p, int lane) {
  CBAR();
  if (lane == 0) __hip_atomic_fetch_add(p, 1u, __ATOMIC_RELAXED, __HIP_MEMORY_SCOPE_WORKGROUP);
  CBAR();
}
__device__ __forceinline__ void lds_spin_ge(LAS unsigned* p, unsigned want) {
#pragma unroll 1
  for (unsigned s = 0; s < 16u * SPIN_CAP; ++s) {
    CBAR();
    if ((unsigned)__builtin_amdgcn_readfirstlane(lds_ld(p)) >= want) break;
  }
  CBAR();
}
__device__ __forceinline__ void wait_all_issued(LAS unsigned* L) {
#pragma unroll 1
  for (unsigned s = 0; s < SPIN_CAP; ++s) {
    CBAR();
    const unsigned e = __builtin_amdgcn_readfirstlane(lds_ld(L + L_FLAG + F_EMPTY));
    const unsigned c = __builtin_amdgcn_readfirstlane(lds_ld(L + L_FLAG + F_CLAIMED));
    const unsigned n = __builtin_amdgcn_readfirstlane(lds_ld(L + L_FLAG + F_ISS));
    if (e && n + WO_EARLY >= 4 * (NS + c)) break;
    __builtin_amdgcn_s_sleep(LSLEEP);
  }
  CBAR();
}
__device__ __forceinline__ void wait_done(LAS unsigned* L, unsigned slack, unsigned cap = SPIN_CAP) {
#pragma unroll 1
  for (unsigned s = 0; s < cap; ++s) {
    CBAR();
    const unsigned e = __builtin_amdgcn_readfirstlane(lds_ld(L + L_FLAG + F_EMPTY));
    const unsigned c = __builtin_amdgcn_readfirstlane(lds_ld(L + L_FLAG + F_CLAIMED));
    const unsigned n = __builtin_amdgcn_readfirstlane(lds_ld(L + L_FLAG + F_DONE));
    if (e && n + slack >= 1u + c) break;
    __builtin_amdgcn_s_sleep(LSLEEP);
  }
  CBAR();
}
__device__ __forceinline__ void st_gran2(rsrc_t rs, int off, float a, float b, unsigned T) {
  v4u g = {__float_as_uint(a), T, __float_as_uint(b), T};
  __builtin_amdgcn_raw_buffer_store_b128(g, rs, off, 0, AUX_SC1);
}
__device__ __forceinline__ void st_gran3(rsrc_t rs, int off, float a, float b, float c, unsigned T) {
  v4u g = {__float_as_uint(a), __float_as_uint(b), __float_as_uint(c), T};
  __builtin_amdgcn_raw_buffer_store_b128(g, rs, off, 0, AUX_SC1);
}
template <int N, bool PAIR, int SLEEP>
__device__ __forceinline__ void gather(rsrc_t rs, int off, int stride, unsigned T, v4u (&gr)[N]) {
  bool miss = true;
#pragma unroll 1
  for (unsigned s2 = 0; s2 < SPIN_CAP; ++s2) {
    CBAR();
    if (miss) {
#pragma unroll
      for (int i = 0; i < N; ++i) gr[i] = __builtin_amdgcn_raw_buffer_load_b128(rs, off + i * stride, 0, AUX_SC1);
    }
    unsigned bad = 0u;
#pragma unroll
    for (int i = 0; i < N; ++i) bad |= PAIR ? ((gr[i][1] ^ T) | (gr[i][3] ^ T)) : (gr[i][3] ^ T);
    miss = bad != 0u;
    if (!__any(miss)) break;
    __builtin_amdgcn_s_sleep(SLEEP);
  }
  CBAR();
}
template <bool PAIR>
__device__ __forceinline__ v4u poll1(rsrc_t rs, int off, unsigned T) {
  CBAR();
  v4u g = __builtin_amdgcn_raw_buffer_load_b128(rs, off, 0, AUX_SC1);
#pragma unroll 1
  for (unsigned s2 = 0; s2 < SPIN_CAP; ++s2) {
    const bool miss = PAIR ? (((g[1] ^ T) | (g[3] ^ T)) != 0u) : (g[3] != T);
    if (!__any(miss)) break;
    __builtin_amdgcn_s_sleep(PSLEEP);
    CBAR();
    if (miss) g = __builtin_amdgcn_raw_buffer_load_b128(rs, off, 0, AUX_SC1);
  }
  CBAR();
  return g;
}
template <int NI>
__device__ __forceinline__ void gemv_rows(const v4u (&wr)[NI], unsigned xv, int lane, float& r0, float& r1) {
  float acc[8] = {0.f, 0.f, 0.f, 0.f, 0.f, 0.f, 0.f, 0.f};
  const int r = lane >> 3;
#pragma unroll
  for (int i = 0; i < NI; ++i) {
    const unsigned hv = (unsigned)__builtin_amdgcn_ds_bpermute((4 * i + (r >> 1)) * 4, (int)xv);
    const unsigned lo = (r & 1) ? hv >> 16 : hv & 0xffffu;
    fma8b(acc, lo, lo << 16, wr[i]);
  }
  red_rows8(acc, r0, r1);
}
#define STAMP(k) do {} while (0)
#define STAMP_FLUSH() do {} while (0)
struct Params {
  const unsigned short *x, *kc, *vc, *wq, *wo, *wg, *wu, *wd, *gw;
  unsigned short* out;
  unsigned char* ws;
};

__global__ void __launch_bounds__(NTHR, 4) fused_decode(Params p) {
  __shared__ __attribute__((aligned(16))) unsigned lds_raw[L_TOTAL];
  LAS unsigned* L = (LAS unsigned*)lds_raw;
  LAS float* Lf = (LAS float*)lds_raw;
  const int b = blockIdx.x, tid = threadIdx.x, lane = tid & 63;
  const int w = __builtin_amdgcn_readfirstlane(tid >> 6);
  STAMP(0);
  unsigned code_touch = 0u;
  if (w == 0) {
    unsigned long long pc; unsigned len;
    asm volatile("s_getpc_b64 %0\n\t.Lmk_code_a:\n\ts_mov_b32 %1, .Lmk_code_z-.Lmk_code_a" : "=s"(pc), "=s"(len));
    const rsrc_t rc = mk_rsrc((const void*)pc, (int)len);
    code_touch = __builtin_amdgcn_raw_buffer_load_b32(rc, ((b >> 3) & 7) * 4096 + lane * 64, 0, 0);
  }
  if (tid < 32) L[L_FLAG + tid] = 0u;
#define WO_WAIT() wait_done(L, WO_SLACK, WO_CAP)
  asm volatile("s_waitcnt lgkmcnt(0)" :: "s"(p.x), "s"(p.kc), "s"(p.vc), "s"(p.wq), "s"(p.wo), "s"(p.wg), "s"(p.wu), "s"(p.wd), "s"(p.gw), "s"(p.out), "s"(p.ws) : "memory");
#define WG_BARRIER2() WG_BARRIER()
  const int hd = b >> 3, sp = b & 7;
  const rsrc_t rws = mk_rsrc(p.ws, WS_BYTES);

  const unsigned Eraw = *(const __attribute__((address_space(4))) unsigned*)(p.ws + E_OFF);
#define GET_TAG() ({ unsigned t_; asm volatile("s_add_u32 %0, %1, 1" : "=s"(t_) : "s"(Eraw) : "scc"); t_; })
  const int cc2 = 8 * (lane & 7) + 4 * ((lane >> 4) & 1) + 2 * (lane >> 5);
  const int rc_off = (lane & 7) * 16;
  unsigned T = 0u;

  const int ro = (sp + hd) & 7;
  const bool p1 = w < 2 || (w == 2 && ro >= 6);
  if (p1) {
    const bool isq = ro < 6;
    const int j = hd * 2 + (ro - 6);
    const int blk = isq ? (ro >> 1) : 3 * (j >> 2) + w;
    const int qr = isq ? 2 * (ro & 1) + w : (j & 3);
    const int ld = isq ? HQ * DH : FF;
    const unsigned short* base = isq ? p.wq + hd * DH + 64 * blk : ((blk & 1) ? p.wu : p.wg) + 64 * (blk >> 1);
    const int pub = (isq ? QP_OFF + hd * 6144 : GP_OFF) + (blk * 4 + qr) * 512;
    if (w == 0 && ro == 0 && lane == 0) __builtin_amdgcn_raw_buffer_store_b32(0u, rws, CNT_OFF + hd * 128, 0, AUX_SC1);
    const rsrc_t rq = mk_rsrc(base + (size_t)(160 * qr) * ld, (159 * ld + 64) * 2);
    const unsigned xa = ((const GAS unsigned*)p.x)[80 * qr + (lane < 40 ? lane : 39)], xb = ((const GAS unsigned*)p.x)[80 * qr + 40 + (lane < 40 ? lane : 39)];
    v4u wa[10], wb[10];
#pragma unroll
    for (int i = 0; i < 10; ++i) wa[i] = __builtin_amdgcn_raw_buffer_load_b128(rq, (lane >> 3) * (ld * 2) + rc_off, i * 16 * ld, AUX_NT);
#pragma unroll
    for (int i = 0; i < 10; ++i) wb[i] = __builtin_amdgcn_raw_buffer_load_b128(rq, (lane >> 3) * (ld * 2) + rc_off, (10 + i) * 16 * ld, AUX_NT);
    WG_BARRIER2();
    float r0, r1, r2, r3;
    gemv_rows<10>(wa, xa, lane, r0, r1);
    gemv_rows<10>(wb, xb, lane, r2, r3);
    asm volatile("" :: "v"(code_touch));
    T = GET_TAG();
    asm volatile("s_waitcnt vmcnt(0)" ::: "memory");
    if (!(lane & 8)) st_gran2(rws, pub + cc2 * 8, r0 + r2, r1 + r3, T);
  } else if (w < 4) {
    WG_BARRIER2();
    T = GET_TAG();
  }
  const int fc = (b >> 1) / 5, fk = 5 * (b & 1) + (b >> 1) % 5;
  const bool has_ffn = b < 240;
  if (w == 3) {
    STAMP(1);
    __builtin_amdgcn_s_sleep(QDELAY);
    {
      v4u gr[6];
      gather<6, true, 8>(rws, QP_OFF + hd * 6144 + lane * 16, 1024, T, gr);
#pragma unroll
      for (int c = 0; c < 3; ++c) {
        const float a0 = __uint_as_float(gr[2 * c][0]) + __uint_as_float(gr[2 * c + 1][0]), a1 = __uint_as_float(gr[2 * c][2]) + __uint_as_float(gr[2 * c + 1][2]);
        const float q0 = swap32_add(a0, a0), q1 = swap32_add(a1, a1);
        if (lane < 32) L[L_Q + 32 * c + lane] = bfbits(q0) | (bfbits(q1) << 16);
      }
    }
    lds_arrive(L + L_FLAG + F_Q, lane);
    STAMP(2);
    float z = 0.f;
#pragma unroll
    for (int i = 0; i < 5; ++i) {
      const unsigned xv = ((const GAS unsigned*)p.x)[lane + 64 * i], gv = ((const GAS unsigned*)p.gw)[lane + 64 * i];
      z += rbf(bf_lo(xv) * bf_lo(gv)); z += rbf(bf_hi(xv) * bf_hi(gv));
    }
    z = rbf(sum_rows(sum8(z)));
    const float gate = rbf(1.f / (1.f + __expf(-z)));
    if (lane == 0) { Lf[L_G] = gate; Lf[L_G + 1] = rbf(1.f - gate); }
    lds_arrive(L + L_FLAG + F_G, lane);
    STAMP(3);
  } else if (w == 0 && has_ffn) {
    lds_wait_ge(L + L_FLAG + F_ISS, 4 * NS - 4);
    unsigned hp;
    {
      v4u gr[4];
      gather<4, true, 16>(rws, GP_OFF + fc * 4096 + lane * 16, 1024, T, gr);
      const float ga = __uint_as_float(gr[0][0]) + __uint_as_float(gr[1][0]), gb = __uint_as_float(gr[0][2]) + __uint_as_float(gr[1][2]);
      const float ua = __uint_as_float(gr[2][0]) + __uint_as_float(gr[3][0]), ub = __uint_as_float(gr[2][2]) + __uint_as_float(gr[3][2]);
      const float g0 = rbf(swap32_add(ga, ga)), g1 = rbf(swap32_add(gb, gb)), u0 = rbf(swap32_add(ua, ua)), u1 = rbf(swap32_add(ub, ub));
      hp = bfbits((g0 > 0.f ? g0 : 0.f) * u0) | (bfbits((g1 > 0.f ? g1 : 0.f) * u1) << 16);
    }
    STAMP(2);
    wait_all_issued(L);
    const rsrc_t rwd = mk_rsrc(p.wd + (size_t)(64 * fc) * D + 64 * fk, (63 * D + 64) * 2);
    v4u wr[8];
#pragma unroll
    for (int i = 0; i < 8; ++i) wr[i] = __builtin_amdgcn_raw_buffer_load_b128(rwd, (lane >> 3) * (D * 2) + rc_off, i * 8 * D * 2, AUX_NT);
    __builtin_amdgcn_sched_barrier(0);
    float r2, r3;
    gemv_rows<8>(wr, hp, lane, r2, r3);
    lds_wait_ge(L + L_FLAG + F_G, 1u);
    const float omg = Lf[L_G + 1];
    if (!(lane & 8)) { Lf[L_BB + cc2] = omg * r2; Lf[L_BB + cc2 + 1] = omg * r3; }
    lds_arrive(L + L_FLAG + F_B, lane);
    STAMP(3);
  }

  static_assert(NS == 13, "unit 12 goes to wave 2 (no projection task) or wave 1");
  int su = w >= 4 ? w - 4 : (w == (ro < 6 ? 2 : 1) ? 12 : -1);
  const int koff = (lane >> 3) * 384 + rc_off;
  const int dgr = cc2 + ((lane >> 3) & 1);
  const bool no_units = w == 0;
#pragma unroll 1
  for (; !no_units;) {
    int key0, part;
    bool half = false;
    if (su >= 0) {
      key0 = sp * (NS * UK) + su * UK; part = -1;
    } else {
      const unsigned tk = lds_arrive(L + L_FLAG + F_TICKET, lane);
      bool empty = false;
#pragma unroll 1
      for (unsigned s = 0; s < SPIN_CAP; ++s) {
        CBAR();
        const unsigned e = __builtin_amdgcn_readfirstlane(lds_ld(L + L_FLAG + F_EMPTY));
        const unsigned n = __builtin_amdgcn_readfirstlane(lds_ld(L + L_FLAG + F_ISS));
        const unsigned q = __builtin_amdgcn_readfirstlane(lds_ld(L + L_FLAG + F_Q));
        if (e) { empty = true; break; }
        if (q && n + LOOKAHEAD >= 4 * (NS + tk)) break;
        __builtin_amdgcn_s_sleep(LSLEEP);
      }
      if (empty) break;
      unsigned u = 0;
      if (lane == 0) u = __hip_atomic_fetch_add((GAS unsigned*)(p.ws + CNT_OFF + hd * 128), 1u, __ATOMIC_RELAXED, __HIP_MEMORY_SCOPE_AGENT);
      u = __builtin_amdgcn_readfirstlane(u);
      if (u >= (unsigned)NP) {
        lds_arrive(L + L_FLAG + F_EMPTY, lane); break; }
      half = u >= (unsigned)NPF;
      key0 = 8 * NS * UK + (half ? NPF * UK + ((int)u - NPF) * (UK / 2) : (int)u * UK); part = 8 + (int)u;
      if (half && lane == 0) __hip_atomic_fetch_add(L + L_FLAG + F_ISS, 2u, __ATOMIC_RELAXED, __HIP_MEMORY_SCOPE_WORKGROUP);
      lds_arrive(L + L_FLAG + F_CLAIMED, lane);
      lds_wait_ge(L + L_FLAG + F_ISS_S, NS);
      STAMP(4);
    }
    const size_t kv_off = ((size_t)hd * SK + (SK - WIN) + key0) * DH;
    const rsrc_t rk = mk_rsrc(p.kc + kv_off, half ? UK * DH : UK * DH * 2);
    const rsrc_t rv = mk_rsrc(p.vc + kv_off, half ? UK * DH : UK * DH * 2);
    v4u kr[12], vr[12];
    if (part < 0 && w >= 4) WG_BARRIER2();
#define TICK(n) do { if (lane == 0) __hip_atomic_fetch_add(L + L_FLAG + F_ISS, (n), __ATOMIC_RELAXED, __HIP_MEMORY_SCOPE_WORKGROUP); __builtin_amdgcn_sched_barrier(0); } while (0)
#pragma unroll
    for (int g = 0; g < 4; ++g) {
#pragma unroll
      for (int t = 0; t < 3; ++t) { kr[g * 3 + t] = __builtin_amdgcn_raw_buffer_load_b128(rk, koff + t * 128, g * 3072, AUX_NT); __builtin_amdgcn_sched_barrier(0); }
      if (g == 1) TICK(half ? 0u : 1u);
      if (g == 3) TICK(1u);
    }
#pragma unroll
    for (int i = 0; i < 12; ++i) {
      const int t = i >> 2, g = i & 3;
      vr[g * 3 + t] = __builtin_amdgcn_raw_buffer_load_b128(rv, koff + t * 128, g * 3072, AUX_NT); __builtin_amdgcn_sched_barrier(0);
      if (i == 5) TICK(half ? 0u : 1u);
      if (i == 11) TICK(1u);
    }
    if (part < 0) { if (w >= 4) T = GET_TAG(); lds_arrive(L + L_FLAG + F_ISS_S, lane); STAMP(1); }
    lds_wait_ge(L + L_FLAG + F_Q, 1u);
    float pe[4], mu, lu;
    unsigned pb[4];
    {
      v4u qf[3];
#pragma unroll
      for (int t = 0; t < 3; ++t) qf[t] = *(LAS v4u*)(L + L_Q + 32 * t + 4 * (lane & 7));
      float s[4] = {0.f, 0.f, -INFINITY, -INFINITY};
#pragma unroll
      for (int g = 0; g < 4; ++g) {
        if (g >= 2 && half) break;
        float a = 0.f;
#pragma unroll
        for (int t = 0; t < 3; ++t)
#pragma unroll
          for (int j = 0; j < 4; ++j) a = dot2(kr[g * 3 + t][j], qf[t][j], a);
        s[g] = rbf(rbf(sum8(a)) * 0.07216878364870322f);
      }
      mu = max_rows(fmaxf(fmaxf(s[0], s[1]), fmaxf(s[2], s[3])));
#pragma unroll
      for (int g = 0; g < 4; ++g) { pb[g] = bfbits(__builtin_amdgcn_exp2f((s[g] - mu) * LOG2E)); pe[g] = bf_lo(pb[g]); }
      lu = sum_rows((pe[0] + pe[1]) + (pe[2] + pe[3]));
    }
    PRIO_HI();
    {
      float pm = mu, pl = lu;
      int slot = part;
      if (part < 0) {
        if (lane == 0) { Lf[L_M + su] = mu; Lf[L_L + su] = lu; }
        if (lds_arrive(L + L_FLAG + F_SML, lane) == NS - 1) {
          const float mj = Lf[L_M + (lane < NS ? lane : 0)], lj = Lf[L_L + (lane < NS ? lane : 0)];
          pm = max_rows(max8(mj));
          pl = sum_rows(sum8(lane < NS ? __builtin_amdgcn_exp2f((mj - pm) * LOG2E) * lj : 0.f));
          slot = sp;
        }
      }
      st_gran2(rws, (slot >= 0 && lane == 0) ? ML_OFF + hd * 1024 + slot * 16 : 0x7ffffff0, pm, pl, T);
      __builtin_amdgcn_sched_barrier(0);
    }
    float o3[3];
#pragma unroll
    for (int t = 0; t < 3; ++t) {
      float acc[8] = {0.f, 0.f, 0.f, 0.f, 0.f, 0.f, 0.f, 0.f};
#pragma unroll
      for (int g = 0; g < 4; ++g) {
        if (g >= 2 && half) break;
        fma8b(acc, pb[g], __float_as_uint(pe[g]), vr[g * 3 + t]);
      }
      float r0, r1;
      red_rows8(acc, r0, r1);
      o3[t] = (lane & 8) ? r1 : r0;
      __builtin_amdgcn_sched_barrier(0);
    }
    if (part >= 0) {
      st_gran3(rws, OP_OFF + ((hd * NPART + part) * 64 + dgr) * 16, o3[0], o3[1], o3[2], T);
      PRIO_LO();
      lds_arrive(L + L_FLAG + F_DONE, lane);
      STAMP(5);
      continue;
    }
#pragma unroll
    for (int t = 0; t < 3; ++t) Lf[L_O + (su * 3 + t) * 64 + dgr] = o3[t];
    STAMP(2);
    if (lds_arrive(L + L_FLAG + F_SARR, lane) == NS - 1) {
      const float mj = Lf[L_M + (lane < NS ? lane : 0)];
      const float M = max_rows(max8(mj));
      const float ej = __builtin_amdgcn_exp2f((mj - M) * LOG2E);
      float s0 = 0.f, s1 = 0.f, s2 = 0.f;
#pragma unroll
      for (int u = 0; u < NS; ++u) {
        const float e = __builtin_bit_cast(float, __builtin_amdgcn_readlane(__builtin_bit_cast(int, ej), u));
        s0 = fmaf(e, Lf[L_O + (u * 3 + 0) * 64 + lane], s0);
        s1 = fmaf(e, Lf[L_O + (u * 3 + 1) * 64 + lane], s1);
        s2 = fmaf(e, Lf[L_O + (u * 3 + 2) * 64 + lane], s2);
      }
      st_gran3(rws, OP_OFF + ((hd * NPART + sp) * 64 + lane) * 16, s0, s1, s2, T);
      lds_arrive(L + L_FLAG + F_DONE, lane);
      STAMP(3);
    }
    PRIO_LO();
    su = -1;
  }

  const int jw = no_units ? 16 : (int)lds_arrive(L + L_FLAG + F_SLOT, lane);
  const int rg = sp >> 1, ch = sp & 1;
  if (jw < 5) {
    wait_all_issued(L);
    WO_WAIT();
    STAMP(4);
    const int cb = 5 * ch + jw;
    const rsrc_t rwo = mk_rsrc(p.wo + (size_t)(hd * DH + 16 * rg) * D + 64 * cb, ((128 + 15) * D + 64) * 2);
    v2f wf[6][4];
    {
      v4u wr[6];
#pragma unroll
      for (int i = 0; i < 6; ++i) wr[i] = __builtin_amdgcn_raw_buffer_load_b128(rwo, (lane >> 3) * (D * 2) + rc_off, ((i % 3) * 64 + (i / 3) * 8) * D * 2, AUX_NT);
#pragma unroll
      for (int i = 0; i < 6; ++i)
#pragma unroll
        for (int j = 0; j < 4; ++j) { wf[i][j] = v2f{bf_lo(wr[i][j]), bf_hi(wr[i][j])}; asm volatile("" : "+v"(wf[i][j])); }
    }
    int yoff = (lane & 8) ? 0x7ffffff0 : YP_OFF + ((hd * 4 + rg) * 640 + 64 * cb + cc2) * 8;
    int gsa = (L_GS + (lane >> 3) * 4) * 4;
    asm volatile("" : "+v"(yoff), "+v"(gsa));
    float y0 = 0.f, y1 = 0.f;
    if (has_ffn && cb == fk) {
      lds_wait_ge(L + L_FLAG + F_B, 1u);
      y0 = Lf[L_BB + cc2]; y1 = Lf[L_BB + cc2 + 1];
    }
    lds_wait_ge(L + L_FLAG + F_G, 1u);
    const float gate = Lf[L_G];
    STAMP(6);
    lds_wait_ge(L + L_FLAG + F_W, TIGHT_AT);
    PRIO_HI();
    lds_spin_ge(L + L_FLAG + F_W, NG);
    v4f ga[NG], gb[NG];
#pragma unroll
    for (int i = 0; i < NG; ++i) { ga[i] = *(LAS v4f*)((LAS char*)L + gsa + i * 256); gb[i] = *(LAS v4f*)((LAS char*)L + gsa + i * 256 + 128); }
    float M = ga[0][3];
#pragma unroll
    for (int i = 1; i < NG; ++i) M = fmaxf(M, ga[i][3]);
    v4f oa = {0.f, 0.f, 0.f, 0.f}, ob = {0.f, 0.f, 0.f, 0.f};
#pragma unroll
    for (int i = 0; i < NG; ++i) { const float f = __builtin_amdgcn_exp2f((ga[i][3] - M) * LOG2E); oa += f * ga[i]; ob += f * gb[i]; }
    const float sc = gate * __builtin_amdgcn_rcpf(ob[3]);
    float acc[8];
#pragma unroll
    for (int j = 0; j < 4; ++j) {
      v2f a2 = oa[0] * wf[0][j];
      a2 = __builtin_elementwise_fma(v2f{oa[1], oa[1]}, wf[1][j], a2);
      a2 = __builtin_elementwise_fma(v2f{oa[2], oa[2]}, wf[2][j], a2);
      a2 = __builtin_elementwise_fma(v2f{ob[0], ob[0]}, wf[3][j], a2);
      a2 = __builtin_elementwise_fma(v2f{ob[1], ob[1]}, wf[4][j], a2);
      a2 = __builtin_elementwise_fma(v2f{ob[2], ob[2]}, wf[5][j], a2);
      acc[2 * j] = a2[0]; acc[2 * j + 1] = a2[1];
    }
    float r0, r1;
    red_rows8(acc, r0, r1);
    st_gran2(rws, yoff, fmaf(sc, r0, y0), fmaf(sc, r1, y1), T);
    STAMP(7);
    if (b < NRED) lds_signal(L + L_FLAG + F_YPUB, lane);
  } else if (jw < 5 + NG) {
    const int gi = jw - 5;
    const bool early = gi >= EARLY0 && gi <= EARLYG;
    if (!early) wait_done(L, 0u);
    STAMP(6);
    const int ooff = OP_OFF + ((hd * NPART + 8 * gi + (lane & 7)) * 64 + 16 * rg + (lane >> 3)) * 16;
    const int moff = ML_OFF + hd * 1024 + (8 * gi + (lane & 7)) * 16;
    CBAR();
    v4u mg = __builtin_amdgcn_raw_buffer_load_b128(rws, moff, 0, AUX_SC1);
    v4u o0 = __builtin_amdgcn_raw_buffer_load_b128(rws, ooff, 0, AUX_SC1);
    v4u o1 = __builtin_amdgcn_raw_buffer_load_b128(rws, ooff + 128, 0, AUX_SC1);
#pragma unroll 1
    for (unsigned s2 = 0; s2 < SPIN_CAP; ++s2) {
      const unsigned long long mb = __ballot(((mg[1] ^ T) | (mg[3] ^ T) | (o0[3] ^ T) | (o1[3] ^ T)) != 0u);
      if (mb == 0ull) break;
      const bool miss = ((mb >> (lane & 7)) & 0x0101010101010101ull) != 0ull;
      if (early) __builtin_amdgcn_s_sleep(ESLEEP); else __builtin_amdgcn_s_sleep(1);
      CBAR();
      if (miss) {
        mg = __builtin_amdgcn_raw_buffer_load_b128(rws, moff, 0, AUX_SC1);
        o0 = __builtin_amdgcn_raw_buffer_load_b128(rws, ooff, 0, AUX_SC1);
        o1 = __builtin_amdgcn_raw_buffer_load_b128(rws, ooff + 128, 0, AUX_SC1);
      }
    }
    CBAR();
    const float mj = __uint_as_float(mg[0]), mi = max8(mj);
    const float e = __builtin_amdgcn_exp2f((mj - mi) * LOG2E);
    const v4f va = {sum8(e * __uint_as_float(o0[0])), sum8(e * __uint_as_float(o0[1])), sum8(e * __uint_as_float(o0[2])), mi};
    const v4f vb = {sum8(e * __uint_as_float(o1[0])), sum8(e * __uint_as_float(o1[1])), sum8(e * __uint_as_float(o1[2])), sum8(e * __uint_as_float(mg[2]))};
    if ((lane & 7) == 0) { *(LAS v4f*)(Lf + L_GS + gi * 64 + (lane >> 3) * 4) = va; *(LAS v4f*)(Lf + L_GS + gi * 64 + 32 + (lane >> 3) * 4) = vb; }
    lds_signal(L + L_FLAG + F_W, lane);
    STAMP(5);
  }
  if (b < NRED && jw >= 2 && jw < 10) {
    const int v = jw - 2;
    lds_wait_ge(L + L_FLAG + F_YPUB, 1u);
    v4u gr[2];
    gather<2, true, 1>(rws, YP_OFF + ((16 * v + (lane >> 3)) * 640 + 16 * b + 2 * (lane & 7)) * 8, 8 * 640 * 8, T, gr);
    STAMP(1);
    const float a0 = sum_rows(__uint_as_float(gr[0][0]) + __uint_as_float(gr[1][0])), a1 = sum_rows(__uint_as_float(gr[0][2]) + __uint_as_float(gr[1][2]));
    if (lane < 8) *(LAS v2f*)(Lf + L_FIN + v * 16 + 2 * lane) = v2f{a0, a1};
    unsigned ord = 0;
    CBAR();
    if (lane == 0) ord = __hip_atomic_fetch_add(L + L_FLAG + F_FIN, 1u, __ATOMIC_RELAXED, __HIP_MEMORY_SCOPE_WORKGROUP);
    CBAR();
    if (__builtin_amdgcn_readfirstlane(ord) == 7u) {
      const v2f f2 = *(LAS v2f*)(Lf + L_FIN + 2 * lane);
      const float s0 = sum_rows(f2[0]), s1 = sum_rows(f2[1]);
      if (lane < 8) ((GAS unsigned*)p.out)[8 * b + lane] = bfbits(s0) | (bfbits(s1) << 16);
      if (b == 0 && lane == 0) __builtin_amdgcn_raw_buffer_store_b32(T, rws, E_OFF, 0, AUX_SC1);
      STAMP(2);
    }
  }
  STAMP_FLUSH();
  asm volatile(".Lmk_code_z:");
}

extern "C" void kernel_launch(void* const* d_in, const int* in_sizes, int n_in,
                              void* d_out, int out_size, void* d_ws, size_t ws_size,
                              hipStream_t stream) {
  Params p;
  p.x = (const unsigned short*)d_in[0];
  p.kc = (const unsigned short*)d_in[1];
  p.vc = (const unsigned short*)d_in[2];
  p.wq = (const unsigned short*)d_in[3];
  p.wo = (const unsigned short*)d_in[4];
  p.wg = (const unsigned short*)d_in[6];
  p.wu = (const unsigned short*)d_in[7];
  p.wd = (const unsigned short*)d_in[8];
  p.gw = (const unsigned short*)d_in[9];
  p.out = (unsigned short*)d_out;
  p.ws = (unsigned char*)d_ws;
  fused_decode<<<NWG, NTHR, 0, stream>>>(p);
}
```

```cpp
#include <hip/hip_runtime.h>
#include <hip/hip_bf16.h>
#include <stdint.h>

#define GAS __attribute__((address_space(1)))
#define LAS __attribute__((address_space(3)))
typedef unsigned v4u __attribute__((ext_vector_type(4)));
typedef float v4f __attribute__((ext_vector_type(4)));
typedef float v2f __attribute__((ext_vector_type(2)));
typedef unsigned long long v8q __attribute__((ext_vector_type(8)));
typedef unsigned long long v2q __attribute__((ext_vector_type(2)));
typedef __bf16 bf16x2 __attribute__((ext_vector_type(2)));
typedef __amdgpu_buffer_rsrc_t rsrc_t;

constexpr int D = 640, HQ = 32, DH = 192, SK = 32768, WIN = 4096, FF = 1536;
constexpr int NWG = 256, NTHR = 1024;
constexpr int NRED = 40;
constexpr int UK = 32;
constexpr int NS = 13;
constexpr int NP = WIN / UK - 8 * NS;
constexpr int NPART = 8 + NP;
constexpr int NG = NPART / 8;
static_assert(NPART % 8 == 0, "gather shape");
constexpr unsigned LOOKAHEAD = 7;
constexpr unsigned WO_HOLD = 18;
constexpr int E_OFF = 0;
constexpr int CNT_OFF = 4096;
constexpr int QP_OFF = CNT_OFF + 32 * 128;
constexpr int GP_OFF = QP_OFF + 32 * 6144;
constexpr int ML_OFF = GP_OFF + 24 * 4096;
constexpr int OP_OFF = ML_OFF + 32 * 1024;
constexpr int YP_OFF = OP_OFF + 32 * NPART * 1024;
constexpr int WS_BYTES = YP_OFF + 128 * 640 * 8;
constexpr int OOB = 0x7ffffff0;
constexpr int AUX_NT = 2, AUX_SC1 = 16;
constexpr unsigned SPIN_CAP = 1u << 20;
constexpr float LOG2E = 1.4426950408889634f;

constexpr int F_Q = 0, F_ISS = 1, F_ISS_S = 2, F_TICKET = 3, F_EMPTY = 4, F_SARR = 5, F_G = 6, F_B = 7, F_FIN = 8, F_YPUB = 9,
              F_SML = 10, F_W = 11, F_CLAIMED = 12, F_SLOT = 13, F_DONE = 14;
constexpr int L_Q = 16;
constexpr int L_M = 112;
constexpr int L_L = 128;
constexpr int L_G = 144;
constexpr int L_BB = 148;
constexpr int L_FIN = 212;
constexpr int L_GS = 340;
constexpr int L_O = L_GS + NG * 64;
constexpr int L_TOTAL = L_O + NS * 192;

__device__ __forceinline__ rsrc_t mk_rsrc(const void* p, int bytes) {
  return __builtin_amdgcn_make_buffer_rsrc((void*)p, 0, bytes, 0x00020000);
}
__device__ __forceinline__ float bf_lo(unsigned u) { return __uint_as_float(u << 16); }
__device__ __forceinline__ float bf_hi(unsigned u) { return __uint_as_float(u & 0xffff0000u); }
__device__ __forceinline__ float rbf(float f) { return __bfloat162float(__float2bfloat16(f)); }
__device__ __forceinline__ unsigned bfbits(float f) { return (unsigned)__builtin_bit_cast(unsigned short, __float2bfloat16(f)); }
__device__ __forceinline__ float dot2(unsigned a, unsigned b, float c) {
  return __builtin_amdgcn_fdot2_f32_bf16(__builtin_bit_cast(bf16x2, a), __builtin_bit_cast(bf16x2, b), c, false);
}
template <int CTRL> __device__ __forceinline__ float dpp(float x) {
  return __builtin_bit_cast(float, __builtin_amdgcn_mov_dpp(__builtin_bit_cast(int, x), CTRL, 0xf, 0xf, true));
}
constexpr int XOR1 = 0xB1, XOR2 = 0x4E, XOR7 = 0x141, XOR8 = 0x128;
__device__ __forceinline__ float swap16_add(float a, float b) {
  auto s = __builtin_amdgcn_permlane16_swap(__float_as_uint(a), __float_as_uint(b), false, false);
  return __uint_as_float(s[0]) + __uint_as_float(s[1]);
}
__device__ __forceinline__ float swap32_add(float a, float b) {
  auto s = __builtin_amdgcn_permlane32_swap(__float_as_uint(a), __float_as_uint(b), false, false);
  return __uint_as_float(s[0]) + __uint_as_float(s[1]);
}
__device__ __forceinline__ float swap16_max(float a) {
  auto s = __builtin_amdgcn_permlane16_swap(__float_as_uint(a), __float_as_uint(a), false, false);
  return fmaxf(__uint_as_float(s[0]), __uint_as_float(s[1]));
}
__device__ __forceinline__ float swap32_max(float a) {
  auto s = __builtin_amdgcn_permlane32_swap(__float_as_uint(a), __float_as_uint(a), false, false);
  return fmaxf(__uint_as_float(s[0]), __uint_as_float(s[1]));
}
__device__ __forceinline__ float sum8(float x) { x += dpp<XOR1>(x); x += dpp<XOR2>(x); x += dpp<XOR7>(x); return x; }
__device__ __forceinline__ float max8(float x) { x = fmaxf(x, dpp<XOR1>(x)); x = fmaxf(x, dpp<XOR2>(x)); return fmaxf(x, dpp<XOR7>(x)); }
__device__ __forceinline__ float sum_rows(float x) { x += dpp<XOR8>(x); x = swap16_add(x, x); return swap32_add(x, x); }
__device__ __forceinline__ float max_rows(float x) { x = fmaxf(x, dpp<XOR8>(x)); x = swap16_max(x); return swap32_max(x); }
__device__ __forceinline__ void red_rows8(const float (&a)[8], float& r0, float& r1) {
  float z[4];
#pragma unroll
  for (int j = 0; j < 4; ++j) z[j] = swap16_add(a[j], a[j + 4]);
  r0 = swap32_add(z[0], z[2]); r1 = swap32_add(z[1], z[3]);
  r0 += dpp<XOR8>(r0); r1 += dpp<XOR8>(r1);
}
__device__ __forceinline__ void fma8b(float (&acc)[8], unsigned slo, unsigned shi, v4u w) {
#pragma unroll
  for (int j = 0; j < 4; ++j) { acc[2 * j] = dot2(w[j], slo, acc[2 * j]); acc[2 * j + 1] = dot2(w[j], shi, acc[2 * j + 1]); }
}
#define WG_BARRIER() do { __builtin_amdgcn_sched_barrier(0); asm volatile("s_barrier" ::: "memory"); __builtin_amdgcn_sched_barrier(0); } while (0)
#define PRIO_HI() __builtin_amdgcn_s_setprio(3)
#define PRIO_LO() do { __builtin_amdgcn_sched_barrier(0); __builtin_amdgcn_s_setprio(0); } while (0)
#define CBAR() asm volatile("" ::: "memory")
#define LGKM0() asm volatile("s_waitcnt lgkmcnt(0)" ::: "memory")
__device__ __forceinline__ unsigned lds_get(LAS unsigned* p) {
  return __builtin_amdgcn_readfirstlane(__hip_atomic_load(p, __ATOMIC_RELAXED, __HIP_MEMORY_SCOPE_WORKGROUP));
}
__device__ __forceinline__ void lds_wait_ge(LAS unsigned* p, unsigned want) {
#pragma unroll 1
  for (unsigned s = 0; s < SPIN_CAP; ++s) {
    CBAR();
    if (lds_get(p) >= want) break;
    __builtin_amdgcn_s_sleep(1);
  }
  CBAR();
}
__device__ __forceinline__ void lds_spin_ge(LAS unsigned* p, unsigned want) {
#pragma unroll 1
  for (unsigned s = 0; s < 16u * SPIN_CAP; ++s) {
    CBAR();
    if (lds_get(p) >= want) break;
  }
  CBAR();
}
__device__ __forceinline__ unsigned lds_arrive(LAS unsigned* p, int lane) {
  LGKM0();
  unsigned old = 0;
  if (lane == 0) old = __hip_atomic_fetch_add(p, 1u, __ATOMIC_RELAXED, __HIP_MEMORY_SCOPE_WORKGROUP);
  old = __builtin_amdgcn_readfirstlane(old);
  CBAR();
  return old;
}
__device__ __forceinline__ void lds_signal(LAS unsigned* p, int lane) {
  CBAR();
  if (lane == 0) __hip_atomic_fetch_add(p, 1u, __ATOMIC_RELAXED, __HIP_MEMORY_SCOPE_WORKGROUP);
  CBAR();
}
__device__ __forceinline__ void wait_all_issued(LAS unsigned* L) {
#pragma unroll 1
  for (unsigned s = 0; s < SPIN_CAP; ++s) {
    CBAR();
    const unsigned e = lds_get(L + F_EMPTY), c = lds_get(L + F_CLAIMED), n = lds_get(L + F_ISS);
    if (e && n >= 4 * (NS + c)) break;
    __builtin_amdgcn_s_sleep(1);
  }
  CBAR();
}
__device__ __forceinline__ void wait_done(LAS unsigned* L, unsigned cap) {
#pragma unroll 1
  for (unsigned s = 0; s < cap; ++s) {
    CBAR();
    const unsigned e = lds_get(L + F_EMPTY), c = lds_get(L + F_CLAIMED), n = lds_get(L + F_DONE);
    if (e && n >= 1u + c) break;
    __builtin_amdgcn_s_sleep(1);
  }
  CBAR();
}
__device__ __forceinline__ void st_gran2(rsrc_t rs, int off, float a, float b, unsigned T) {
  v4u g = {__float_as_uint(a), T, __float_as_uint(b), T};
  __builtin_amdgcn_raw_buffer_store_b128(g, rs, off, 0, AUX_SC1);
}
__device__ __forceinline__ void st_gran3(rsrc_t rs, int off, float a, float b, float c, unsigned T) {
  v4u g = {__float_as_uint(a), __float_as_uint(b), __float_as_uint(c), T};
  __builtin_amdgcn_raw_buffer_store_b128(g, rs, off, 0, AUX_SC1);
}
template <int N, int SLEEP>
__device__ __forceinline__ void gather(rsrc_t rs, int off, int stride, unsigned T, v4u (&gr)[N]) {
  bool miss = true;
#pragma unroll 1
  for (unsigned s2 = 0; s2 < SPIN_CAP; ++s2) {
    CBAR();
    if (miss) {
#pragma unroll
      for (int i = 0; i < N; ++i) gr[i] = __builtin_amdgcn_raw_buffer_load_b128(rs, off + i * stride, 0, AUX_SC1);
    }
    unsigned bad = 0u;
#pragma unroll
    for (int i = 0; i < N; ++i) bad |= (gr[i][1] ^ T) | (gr[i][3] ^ T);
    miss = bad != 0u;
    if (!__any(miss)) break;
    __builtin_amdgcn_s_sleep(SLEEP);
  }
  CBAR();
}
template <int NI>
__device__ __forceinline__ void gemv_rows(const v4u (&wr)[NI], unsigned xv, int lane, float& r0, float& r1) {
  float acc[8] = {0.f, 0.f, 0.f, 0.f, 0.f, 0.f, 0.f, 0.f};
  const int r = lane >> 3;
#pragma unroll
  for (int i = 0; i < NI; ++i) {
    const unsigned hv = (unsigned)__builtin_amdgcn_ds_bpermute((4 * i + (r >> 1)) * 4, (int)xv);
    const unsigned lo = (r & 1) ? hv >> 16 : hv & 0xffffu;
    fma8b(acc, lo, lo << 16, wr[i]);
  }
  red_rows8(acc, r0, r1);
}

struct Params {
  const unsigned short *x, *kc, *vc, *wq, *wo, *wg, *wu, *wd, *gw;
  unsigned short* out;
  unsigned char* ws;
};
static_assert(sizeof(Params) == 88, "argument block read by offset below");

__global__ void __launch_bounds__(NTHR, 4) fused_decode(Params) {
  v8q ka; v2q kb; unsigned long long kc_;
  asm volatile("s_load_dwordx16 %0, %3, 0x0\n\ts_load_dwordx4 %1, %3, 0x40\n\ts_load_dwordx2 %2, %3, 0x50"
               : "=&s"(ka), "=&s"(kb), "=&s"(kc_) : "s"(__builtin_amdgcn_kernarg_segment_ptr()));
  __shared__ __attribute__((aligned(16))) unsigned lds_raw[L_TOTAL];
  LAS unsigned* L = (LAS unsigned*)lds_raw;
  LAS float* Lf = (LAS float*)lds_raw;
  const int b = blockIdx.x, tid = threadIdx.x, lane = tid & 63;
  const int w = __builtin_amdgcn_readfirstlane(tid >> 6);
  unsigned code_touch = 0u;
  if (w == 0) {
    unsigned long long pc; unsigned len;
    asm volatile("s_getpc_b64 %0\n\t.Lfd_code_a:\n\ts_mov_b32 %1, .Lfd_code_z-.Lfd_code_a" : "=s"(pc), "=s"(len));
    code_touch = __builtin_amdgcn_raw_buffer_load_b32(mk_rsrc((const void*)pc, (int)len), ((b >> 3) & 7) * 4096 + lane * 64, 0, 0);
  }
  if (tid < 16) L[tid] = 0u;
  const int hd = b >> 3, sp = b & 7;
  const int ro = (sp + hd) & 7;
  const bool isq = ro < 6;
  const int jf = hd * 2 + (ro - 6);
  int blk = isq ? (ro >> 1) : 3 * (jf >> 2) + w;
  int qr = isq ? 2 * (ro & 1) + w : (jf & 3);
  int ld = isq ? HQ * DH : FF;
  int p1off = (160 * qr * ld + (isq ? hd * DH + 64 * blk : 64 * (blk >> 1))) * 2;
  int p1v = (lane >> 3) * (ld * 2) + (lane & 7) * 16;
  asm volatile("" : "+s"(blk), "+s"(qr), "+s"(ld), "+s"(p1off));
  asm volatile("" : "+v"(p1v));
  asm volatile("s_waitcnt lgkmcnt(0)" : "+s"(ka), "+s"(kb), "+s"(kc_) :: "memory");
  Params p;
  p.x = (const unsigned short*)ka[0]; p.kc = (const unsigned short*)ka[1]; p.vc = (const unsigned short*)ka[2]; p.wq = (const unsigned short*)ka[3];
  p.wo = (const unsigned short*)ka[4]; p.wg = (const unsigned short*)ka[5]; p.wu = (const unsigned short*)ka[6]; p.wd = (const unsigned short*)ka[7];
  p.gw = (const unsigned short*)kb[0]; p.out = (unsigned short*)kb[1]; p.ws = (unsigned char*)kc_;
  const rsrc_t rws = mk_rsrc(p.ws, WS_BYTES);

  const unsigned Eraw = *(const __attribute__((address_space(4))) unsigned*)(p.ws + E_OFF);
#define GET_TAG() ({ unsigned t_; asm volatile("s_add_u32 %0, %1, 1" : "=s"(t_) : "s"(Eraw) : "scc"); t_; })
  unsigned T = 0u;
  const int cc2 = 8 * (lane & 7) + 4 * ((lane >> 4) & 1) + 2 * (lane >> 5);
  const int rc_off = (lane & 7) * 16;

  if (w < 2 || (w == 2 && !isq)) {
    const unsigned char* base = (const unsigned char*)(isq ? p.wq : ((blk & 1) ? p.wu : p.wg)) + p1off;
    const rsrc_t rq = mk_rsrc(base, (159 * ld + 64) * 2);
    v4u wa[10], wb[10];
#pragma unroll
    for (int i = 0; i < 10; ++i) wa[i] = __builtin_amdgcn_raw_buffer_load_b128(rq, p1v, i * 16 * ld, AUX_NT);
#pragma unroll
    for (int i = 0; i < 10; ++i) wb[i] = __builtin_amdgcn_raw_buffer_load_b128(rq, p1v, (10 + i) * 16 * ld, AUX_NT);
    const unsigned xa = ((const GAS unsigned*)p.x)[80 * qr + (lane < 40 ? lane : 39)], xb = ((const GAS unsigned*)p.x)[80 * qr + 40 + (lane < 40 ? lane : 39)];
    if (w == 0 && ro == 0 && lane == 0) __builtin_amdgcn_raw_buffer_store_b32(0u, rws, CNT_OFF + hd * 128, 0, AUX_SC1);
    WG_BARRIER();
    const int pub = (isq ? QP_OFF + hd * 6144 : GP_OFF) + (blk * 4 + qr) * 512;
    float r0, r1, r2, r3;
    gemv_rows<10>(wa, xa, lane, r0, r1);
    gemv_rows<10>(wb, xb, lane, r2, r3);
    asm volatile("" :: "v"(code_touch));
    T = GET_TAG();
    asm volatile("s_waitcnt vmcnt(0)" ::: "memory");
    if (!(lane & 8)) st_gran2(rws, pub + cc2 * 8, r0 + r2, r1 + r3, T);
  } else if (w < 4) {
    WG_BARRIER();
    T = GET_TAG();
  }
  const int fc = (b >> 1) / 5, fk = 5 * (b & 1) + (b >> 1) % 5;
  const bool has_ffn = b < 240;
  if (w == 3) {
    __builtin_amdgcn_s_sleep(127);
    {
      v4u gr[6];
      gather<6, 8>(rws, QP_OFF + hd * 6144 + lane * 16, 1024, T, gr);
#pragma unroll
      for (int c = 0; c < 3; ++c) {
        const float a0 = __uint_as_float(gr[2 * c][0]) + __uint_as_float(gr[2 * c + 1][0]), a1 = __uint_as_float(gr[2 * c][2]) + __uint_as_float(gr[2 * c + 1][2]);
        const float q0 = swap32_add(a0, a0), q1 = swap32_add(a1, a1);
        if (lane < 32) L[L_Q + 32 * c + lane] = bfbits(q0) | (bfbits(q1) << 16);
      }
    }
    lds_arrive(L + F_Q, lane);
    float z = 0.f;
#pragma unroll
    for (int i = 0; i < 5; ++i) {
      const unsigned xv = ((const GAS unsigned*)p.x)[lane + 64 * i], gv = ((const GAS unsigned*)p.gw)[lane + 64 * i];
      z += rbf(bf_lo(xv) * bf_lo(gv)); z += rbf(bf_hi(xv) * bf_hi(gv));
    }
    z = rbf(sum_rows(sum8(z)));
    const float gate = rbf(1.f / (1.f + __expf(-z)));
    if (lane == 0) { Lf[L_G] = gate; Lf[L_G + 1] = rbf(1.f - gate); }
    lds_arrive(L + F_G, lane);
  } else if (w == 0 && has_ffn) {
    lds_wait_ge(L + F_ISS, 4 * NS - 4);
    unsigned hp;
    {
      v4u gr[4];
      gather<4, 16>(rws, GP_OFF + fc * 4096 + lane * 16, 1024, T, gr);
      const float ga = __uint_as_float(gr[0][0]) + __uint_as_float(gr[1][0]), gb = __uint_as_float(gr[0][2]) + __uint_as_float(gr[1][2]);
      const float ua = __uint_as_float(gr[2][0]) + __uint_as_float(gr[3][0]), ub = __uint_as_float(gr[2][2]) + __uint_as_float(gr[3][2]);
      const float g0 = rbf(swap32_add(ga, ga)), g1 = rbf(swap32_add(gb, gb)), u0 = rbf(swap32_add(ua, ua)), u1 = rbf(swap32_add(ub, ub));
      hp = bfbits((g0 > 0.f ? g0 : 0.f) * u0) | (bfbits((g1 > 0.f ? g1 : 0.f) * u1) << 16);
    }
    wait_all_issued(L);
    const rsrc_t rwd = mk_rsrc(p.wd + (size_t)(64 * fc) * D + 64 * fk, (63 * D + 64) * 2);
    v4u wr[8];
#pragma unroll
    for (int i = 0; i < 8; ++i) wr[i] = __builtin_amdgcn_raw_buffer_load_b128(rwd, (lane >> 3) * (D * 2) + rc_off, i * 8 * D * 2, AUX_NT);
    __builtin_amdgcn_sched_barrier(0);
    float r2, r3;
    gemv_rows<8>(wr, hp, lane, r2, r3);
    lds_wait_ge(L + F_G, 1u);
    const float omg = Lf[L_G + 1];
    if (!(lane & 8)) { Lf[L_BB + cc2] = omg * r2; Lf[L_BB + cc2 + 1] = omg * r3; }
    lds_arrive(L + F_B, lane);
  }

  int su = w >= 4 ? w - 4 : (w == (isq ? 2 : 1) ? 12 : -1);
  const int koff = (lane >> 3) * 384 + rc_off;
  const int dgr = cc2 + ((lane >> 3) & 1);
  const bool no_units = w == 0;
#pragma unroll 1
  for (; !no_units;) {
    int key0, part;
    if (su >= 0) {
      key0 = sp * (NS * UK) + su * UK; part = -1;
    } else {
      const unsigned tk = lds_arrive(L + F_TICKET, lane);
      bool empty = false;
#pragma unroll 1
      for (unsigned s = 0; s < SPIN_CAP; ++s) {
        CBAR();
        const unsigned e = lds_get(L + F_EMPTY), n = lds_get(L + F_ISS), q = lds_get(L + F_Q);
        if (e) { empty = true; break; }
        if (q && n + LOOKAHEAD >= 4 * (NS + tk)) break;
        __builtin_amdgcn_s_sleep(1);
      }
      if (empty) break;
      unsigned u = 0;
      if (lane == 0) u = __hip_atomic_fetch_add((GAS unsigned*)(p.ws + CNT_OFF + hd * 128), 1u, __ATOMIC_RELAXED, __HIP_MEMORY_SCOPE_AGENT);
      u = __builtin_amdgcn_readfirstlane(u);
      if (u >= (unsigned)NP) { lds_arrive(L + F_EMPTY, lane); break; }
      key0 = 8 * NS * UK + (int)u * UK; part = 8 + (int)u;
      lds_arrive(L + F_CLAIMED, lane);
      lds_wait_ge(L + F_ISS_S, NS);
    }
    const size_t kv_off = ((size_t)hd * SK + (SK - WIN) + key0) * DH;
    const rsrc_t rk = mk_rsrc(p.kc + kv_off, UK * DH * 2);
    const rsrc_t rv = mk_rsrc(p.vc + kv_off, UK * DH * 2);
    v4u kr[12], vr[12];
    if (part < 0 && w >= 4) WG_BARRIER();
#define TICK() do { if (lane == 0) __hip_atomic_fetch_add(L + F_ISS, 1u, __ATOMIC_RELAXED, __HIP_MEMORY_SCOPE_WORKGROUP); __builtin_amdgcn_sched_barrier(0); } while (0)
#pragma unroll
    for (int g = 0; g < 4; ++g) {
#pragma unroll
      for (int t = 0; t < 3; ++t) { kr[g * 3 + t] = __builtin_amdgcn_raw_buffer_load_b128(rk, koff + t * 128, g * 3072, AUX_NT); __builtin_amdgcn_sched_barrier(0); }
      if (g & 1) TICK();
    }
#pragma unroll
    for (int i = 0; i < 12; ++i) {
      const int t = i >> 2, g = i & 3;
      vr[g * 3 + t] = __builtin_amdgcn_raw_buffer_load_b128(rv, koff + t * 128, g * 3072, AUX_NT); __builtin_amdgcn_sched_barrier(0);
      if (i == 5 || i == 11) TICK();
    }
    if (part < 0) { if (w >= 4) T = GET_TAG(); lds_arrive(L + F_ISS_S, lane); }
    lds_wait_ge(L + F_Q, 1u);
    float pe[4], mu, lu;
    unsigned pb[4];
    {
      v4u qf[3];
#pragma unroll
      for (int t = 0; t < 3; ++t) qf[t] = *(LAS v4u*)(L + L_Q + 32 * t + 4 * (lane & 7));
      float s[4];
#pragma unroll
      for (int g = 0; g < 4; ++g) {
        float a = 0.f;
#pragma unroll
        for (int t = 0; t < 3; ++t)
#pragma unroll
          for (int j = 0; j < 4; ++j) a = dot2(kr[g * 3 + t][j], qf[t][j], a);
        s[g] = rbf(rbf(sum8(a)) * 0.07216878364870322f);
      }
      mu = max_rows(fmaxf(fmaxf(s[0], s[1]), fmaxf(s[2], s[3])));
#pragma unroll
      for (int g = 0; g < 4; ++g) { pb[g] = bfbits(__builtin_amdgcn_exp2f((s[g] - mu) * LOG2E)); pe[g] = bf_lo(pb[g]); }
      lu = sum_rows((pe[0] + pe[1]) + (pe[2] + pe[3]));
    }
    PRIO_HI();
    {
      float pm = mu, pl = lu;
      int slot = part;
      if (part < 0) {
        if (lane == 0) { Lf[L_M + su] = mu; Lf[L_L + su] = lu; }
        if (lds_arrive(L + F_SML, lane) == NS - 1) {
          const float mj = Lf[L_M + (lane < NS ? lane : 0)], lj = Lf[L_L + (lane < NS ? lane : 0)];
          pm = max_rows(max8(mj));
          pl = sum_rows(sum8(lane < NS ? __builtin_amdgcn_exp2f((mj - pm) * LOG2E) * lj : 0.f));
          slot = sp;
        }
      }
      st_gran2(rws, (slot >= 0 && lane == 0) ? ML_OFF + hd * 1024 + slot * 16 : OOB, pm, pl, T);
      __builtin_amdgcn_sched_barrier(0);
    }
    float o3[3];
#pragma unroll
    for (int t = 0; t < 3; ++t) {
      float acc[8] = {0.f, 0.f, 0.f, 0.f, 0.f, 0.f, 0.f, 0.f};
#pragma unroll
      for (int g = 0; g < 4; ++g) fma8b(acc, pb[g], __float_as_uint(pe[g]), vr[g * 3 + t]);
      float r0, r1;
      red_rows8(acc, r0, r1);
      o3[t] = (lane & 8) ? r1 : r0;
      __builtin_amdgcn_sched_barrier(0);
    }
    if (part >= 0) {
      st_gran3(rws, OP_OFF + ((hd * NPART + part) * 64 + dgr) * 16, o3[0], o3[1], o3[2], T);
      PRIO_LO();
      lds_arrive(L + F_DONE, lane);
      continue;
    }
#pragma unroll
    for (int t = 0; t < 3; ++t) Lf[L_O + (su * 3 + t) * 64 + dgr] = o3[t];
    if (lds_arrive(L + F_SARR, lane) == NS - 1) {
      const float mj = Lf[L_M + (lane < NS ? lane : 0)];
      const float M = max_rows(max8(mj));
      const float ej = __builtin_amdgcn_exp2f((mj - M) * LOG2E);
      float s0 = 0.f, s1 = 0.f, s2 = 0.f;
#pragma unroll
      for (int u = 0; u < NS; ++u) {
        const float e = __builtin_bit_cast(float, __builtin_amdgcn_readlane(__builtin_bit_cast(int, ej), u));
        s0 = fmaf(e, Lf[L_O + (u * 3 + 0) * 64 + lane], s0);
        s1 = fmaf(e, Lf[L_O + (u * 3 + 1) * 64 + lane], s1);
        s2 = fmaf(e, Lf[L_O + (u * 3 + 2) * 64 + lane], s2);
      }
      st_gran3(rws, OP_OFF + ((hd * NPART + sp) * 64 + lane) * 16, s0, s1, s2, T);
      lds_arrive(L + F_DONE, lane);
    }
    PRIO_LO();
    su = -1;
  }

  const int jw = no_units ? 16 : (int)lds_arrive(L + F_SLOT, lane);
  const int rg = sp >> 1, ch = sp & 1;
  if (jw < 5) {
    wait_all_issued(L);
    wait_done(L, WO_HOLD);
    const int cb = 5 * ch + jw;
    const rsrc_t rwo = mk_rsrc(p.wo + (size_t)(hd * DH + 16 * rg) * D + 64 * cb, ((128 + 15) * D + 64) * 2);
    v2f wf[6][4];
    {
      v4u wr[6];
#pragma unroll
      for (int i = 0; i < 6; ++i) wr[i] = __builtin_amdgcn_raw_buffer_load_b128(rwo, (lane >> 3) * (D * 2) + rc_off, ((i % 3) * 64 + (i / 3) * 8) * D * 2, AUX_NT);
#pragma unroll
      for (int i = 0; i < 6; ++i)
#pragma unroll
        for (int j = 0; j < 4; ++j) { wf[i][j] = v2f{bf_lo(wr[i][j]), bf_hi(wr[i][j])}; asm volatile("" : "+v"(wf[i][j])); }
    }
    int yoff = (lane & 8) ? OOB : YP_OFF + ((hd * 4 + rg) * 640 + 64 * cb + cc2) * 8;
    int gsa = (L_GS + (lane >> 3) * 4) * 4;
    asm volatile("" : "+v"(yoff), "+v"(gsa));
    float y0 = 0.f, y1 = 0.f;
    if (has_ffn && cb == fk) {
      lds_wait_ge(L + F_B, 1u);
      y0 = Lf[L_BB + cc2]; y1 = Lf[L_BB + cc2 + 1];
    }
    lds_wait_ge(L + F_G, 1u);
    const float gate = Lf[L_G];
    lds_wait_ge(L + F_W, 1u);
    PRIO_HI();
    lds_spin_ge(L + F_W, NG);
    v4f ga[NG], gb[NG];
#pragma unroll
    for (int i = 0; i < NG; ++i) { ga[i] = *(LAS v4f*)((LAS char*)L + gsa + i * 256); gb[i] = *(LAS v4f*)((LAS char*)L + gsa + i * 256 + 128); }
    float M = ga[0][3];
#pragma unroll
    for (int i = 1; i < NG; ++i) M = fmaxf(M, ga[i][3]);
    v4f oa = {0.f, 0.f, 0.f, 0.f}, ob = {0.f, 0.f, 0.f, 0.f};
#pragma unroll
    for (int i = 0; i < NG; ++i) { const float f = __builtin_amdgcn_exp2f((ga[i][3] - M) * LOG2E); oa += f * ga[i]; ob += f * gb[i]; }
    const float sc = gate * __builtin_amdgcn_rcpf(ob[3]);
    float acc[8];
#pragma unroll
    for (int j = 0; j < 4; ++j) {
      v2f a2 = oa[0] * wf[0][j];
      a2 = __builtin_elementwise_fma(v2f{oa[1], oa[1]}, wf[1][j], a2);
      a2 = __builtin_elementwise_fma(v2f{oa[2], oa[2]}, wf[2][j], a2);
      a2 = __builtin_elementwise_fma(v2f{ob[0], ob[0]}, wf[3][j], a2);
      a2 = __builtin_elementwise_fma(v2f{ob[1], ob[1]}, wf[4][j], a2);
      a2 = __builtin_elementwise_fma(v2f{ob[2], ob[2]}, wf[5][j], a2);
      acc[2 * j] = a2[0]; acc[2 * j + 1] = a2[1];
    }
    float r0, r1;
    red_rows8(acc, r0, r1);
    st_gran2(rws, yoff, fmaf(sc, r0, y0), fmaf(sc, r1, y1), T);
    if (b < NRED) lds_signal(L + F_YPUB, lane);
  } else if (jw < 5 + NG) {
    const int gi = jw - 5;
    const bool early = gi == 1 || gi == 2;
    if (!early) wait_done(L, SPIN_CAP);
    const int ooff = OP_OFF + ((hd * NPART + 8 * gi + (lane & 7)) * 64 + 16 * rg + (lane >> 3)) * 16;
    const int moff = ML_OFF + hd * 1024 + (8 * gi + (lane & 7)) * 16;
    CBAR();
    v4u mg = __builtin_amdgcn_raw_buffer_load_b128(rws, moff, 0, AUX_SC1);
    v4u o0 = __builtin_amdgcn_raw_buffer_load_b128(rws, ooff, 0, AUX_SC1);
    v4u o1 = __builtin_amdgcn_raw_buffer_load_b128(rws, ooff + 128, 0, AUX_SC1);
#pragma unroll 1
    for (unsigned s2 = 0; s2 < SPIN_CAP; ++s2) {
      const unsigned long long mb = __ballot(((mg[1] ^ T) | (mg[3] ^ T) | (o0[3] ^ T) | (o1[3] ^ T)) != 0u);
      if (mb == 0ull) break;
      const bool miss = ((mb >> (lane & 7)) & 0x0101010101010101ull) != 0ull;
      if (early) __builtin_amdgcn_s_sleep(10); else __builtin_amdgcn_s_sleep(1);
      CBAR();
      if (miss) {
        mg = __builtin_amdgcn_raw_buffer_load_b128(rws, moff, 0, AUX_SC1);
        o0 = __builtin_amdgcn_raw_buffer_load_b128(rws, ooff, 0, AUX_SC1);
        o1 = __builtin_amdgcn_raw_buffer_load_b128(rws, ooff + 128, 0, AUX_SC1);
      }
    }
    CBAR();
    const float mj = __uint_as_float(mg[0]), mi = max8(mj);
    const float e = __builtin_amdgcn_exp2f((mj - mi) * LOG2E);
    const v4f va = {sum8(e * __uint_as_float(o0[0])), sum8(e * __uint_as_float(o0[1])), sum8(e * __uint_as_float(o0[2])), mi};
    const v4f vb = {sum8(e * __uint_as_float(o1[0])), sum8(e * __uint_as_float(o1[1])), sum8(e * __uint_as_float(o1[2])), sum8(e * __uint_as_float(mg[2]))};
    if ((lane & 7) == 0) { *(LAS v4f*)(Lf + L_GS + gi * 64 + (lane >> 3) * 4) = va; *(LAS v4f*)(Lf + L_GS + gi * 64 + 32 + (lane >> 3) * 4) = vb; }
    lds_signal(L + F_W, lane);
  }
  if (b < NRED && jw >= 2 && jw < 10) {
    const int v = jw - 2;
    lds_wait_ge(L + F_YPUB, 1u);
    v4u gr[2];
    gather<2, 1>(rws, YP_OFF + ((16 * v + (lane >> 3)) * 640 + 16 * b + 2 * (lane & 7)) * 8, 8 * 640 * 8, T, gr);
    const float a0 = sum_rows(__uint_as_float(gr[0][0]) + __uint_as_float(gr[1][0])), a1 = sum_rows(__uint_as_float(gr[0][2]) + __uint_as_float(gr[1][2]));
    if (lane < 8) *(LAS v2f*)(Lf + L_FIN + v * 16 + 2 * lane) = v2f{a0, a1};
    unsigned ord = 0;
    CBAR();
    if (lane == 0) ord = __hip_atomic_fetch_add(L + F_FIN, 1u, __ATOMIC_RELAXED, __HIP_MEMORY_SCOPE_WORKGROUP);
    CBAR();
    if (__builtin_amdgcn_readfirstlane(ord) == 7u) {
      const v2f f2 = *(LAS v2f*)(Lf + L_FIN + 2 * lane);
      const float s0 = sum_rows(f2[0]), s1 = sum_rows(f2[1]);
      if (lane < 8) ((GAS unsigned*)p.out)[8 * b + lane] = bfbits(s0) | (bfbits(s1) << 16);
      if (b == 0 && lane == 0) __builtin_amdgcn_raw_buffer_store_b32(T, rws, E_OFF, 0, AUX_SC1);
    }
  }
  asm volatile(".Lfd_code_z:");
}

extern "C" void kernel_launch(void* const* d_in, const int* in_sizes, int n_in,
                              void* d_out, int out_size, void* d_ws, size_t ws_size,
                              hipStream_t stream) {
  Params p;
  p.x = (const unsigned short*)d_in[0];
  p.kc = (const unsigned short*)d_in[1];
  p.vc = (const unsigned short*)d_in[2];
  p.wq = (const unsigned short*)d_in[3];
  p.wo = (const unsigned short*)d_in[4];
  p.wg = (const unsigned short*)d_in[6];
  p.wu = (const unsigned short*)d_in[7];
  p.wd = (const unsigned short*)d_in[8];
  p.gw = (const unsigned short*)d_in[9];
  p.out = (unsigned short*)d_out;
  p.ws = (unsigned char*)d_ws;
  fused_decode<<<NWG, NTHR, 0, stream>>>(p);
}
```
